```python
import torch

def _softmax(x, axis=-1):
    return torch.softmax(x, dim=axis)
def _gelu(x):
    return torch.nn.functional.gelu(x, approximate="tanh")
def _silu(x):
    return torch.nn.functional.silu(x)
def _sigmoid(x):
    return torch.sigmoid(x)
def _layernorm(x, axis=-1, eps=1e-5):
    m = x.mean(dim=axis, keepdim=True)
    v = x.var(dim=axis, keepdim=True, unbiased=False)
    return (x - m) / torch.sqrt(v + eps)
def _scatter_add(tgt, idx, src, axis=0):
    return tgt.clone().index_add_(axis, idx, src)
def _segment_sum(src, seg, n):
    out = torch.zeros((n,) + src.shape[1:], dtype=src.dtype)
    return out.index_add_(0, seg, src)
def _pad(x, pw):
    flat = tuple(v for pair in reversed(pw) for v in pair)
    return torch.nn.functional.pad(x, flat)

kernel_name = 'standalone__dense_activation_squared_relu__sampling'

def setup_inputs():

    _w = (lambda a: (lambda t: t.to(torch.bfloat16) if t.is_floating_point() else t)(torch.as_tensor(a)))
    B, S, D, F = 32, 16, 576, 4608
    return {
        "x": _w((lambda *s: torch.randn(*s, dtype=torch.bfloat16) * 0.05)(B, S, D)),
        "w1": _w((lambda *s: torch.randn(*s, dtype=torch.bfloat16) * 0.05)(D, F)),
        "b1": _w((lambda *s: torch.randn(*s, dtype=torch.bfloat16) * 0.05)(F)),
        "w2": _w((lambda *s: torch.randn(*s, dtype=torch.bfloat16) * 0.05)(F, D)),
        "b2": _w((lambda *s: torch.randn(*s, dtype=torch.bfloat16) * 0.05)(D)),
    }

def reference(x, w1, b1, w2, b2):
    h = torch.einsum("bsd,df->bsf", x, w1) + b1
    zero = torch.zeros_like(h)
    r = torch.where(h > zero, h, zero)
    h = r * r
    out = torch.einsum("bsf,fd->bsd", h, w2) + b2
    return out
```

```cpp
#include <hip/hip_runtime.h>
#include <hip/hip_bf16.h>
typedef short bf16x8 __attribute__((ext_vector_type(8)));
typedef __bf16 b16x8 __attribute__((ext_vector_type(8)));
typedef float f32x4 __attribute__((ext_vector_type(4)));
typedef unsigned short u16;
__device__ __forceinline__ float bf2f(u16 v) { return __uint_as_float((unsigned)v << 16); }
__device__ __forceinline__ u16 f2bf(float f) { __bf16 b = (__bf16)f; return __builtin_bit_cast(u16, b); }

template <int K, int N, bool FIRST>
__global__ void __launch_bounds__(64) gemm(const u16* __restrict__ A, const u16* __restrict__ B, const u16* __restrict__ bias, u16* __restrict__ C) {
  const int l = threadIdx.x, tn = blockIdx.x, tm = blockIdx.y;
  f32x4 acc = {0, 0, 0, 0};
  for (int k0 = 0; k0 < K; k0 += 32) {
    b16x8 a = *(const b16x8*)(A + (size_t)(tm * 16 + (l & 15)) * K + k0 + 8 * (l >> 4));
    bf16x8 b;
#pragma unroll
    for (int j = 0; j < 8; ++j) b[j] = (short)B[(size_t)(k0 + 8 * (l >> 4) + j) * N + tn * 16 + (l & 15)];
    acc = __builtin_amdgcn_mfma_f32_16x16x32_bf16(a, __builtin_bit_cast(b16x8, b), acc, 0, 0, 0);
  }
  const int n = tn * 16 + (l & 15);
  const float bv = bf2f(bias[n]);
#pragma unroll
  for (int r = 0; r < 4; ++r) {
    const int m = tm * 16 + 4 * (l >> 4) + r;
    float v = bf2f(f2bf(acc[r]));
    v = bf2f(f2bf(v + bv));
    if (FIRST) { v = v > 0.f ? v : 0.f; v = v * v; }
    C[(size_t)m * N + n] = f2bf(v);
  }
}
extern "C" void kernel_launch(void* const* d_in, const int* in_sizes, int n_in, void* d_out, int out_size, void* d_ws, size_t ws_size, hipStream_t stream) {
  const u16* x = (const u16*)d_in[0]; const u16* w1 = (const u16*)d_in[1]; const u16* b1 = (const u16*)d_in[2];
  const u16* w2 = (const u16*)d_in[3]; const u16* b2 = (const u16*)d_in[4];
  u16* h = (u16*)d_ws;
  gemm<576, 4608, true><<<dim3(288, 32), 64, 0, stream>>>(x, w1, b1, h);
  gemm<4608, 576, false><<<dim3(36, 32), 64, 0, stream>>>(h, w2, b2, (u16*)d_out);
}
```

```cpp
#include <hip/hip_runtime.h>

typedef unsigned short u16;
typedef unsigned int u32;
typedef unsigned long long u64;
typedef __bf16 bf16x8 __attribute__((ext_vector_type(8)));
typedef __bf16 bf16x2 __attribute__((ext_vector_type(2)));
typedef short s16x4 __attribute__((ext_vector_type(4)));
typedef short s16x8 __attribute__((ext_vector_type(8)));
typedef float f32x4 __attribute__((ext_vector_type(4)));
typedef float f32x2 __attribute__((ext_vector_type(2)));
typedef u32 u32x4 __attribute__((ext_vector_type(4)));
typedef u32 u32x2 __attribute__((ext_vector_type(2)));
typedef int i32x4 __attribute__((ext_vector_type(4)));
#define AS3 __attribute__((address_space(3)))
#define AS4 __attribute__((address_space(4)))
typedef AS3 char* lptr;

constexpr int SLOT = 26 * 1024;
constexpr int LDS_FLAGS = 6 * SLOT;
constexpr int LDS_BYTES = LDS_FLAGS + 256;
constexpr u32 WS_H = 4096;
constexpr u32 WS_P = WS_H + 7u * 4 * 5 * 36 * 1024;
constexpr u32 WS_END = WS_P + 7u * 9 * 16 * 5 * 1024;
constexpr int SPIN_MAX = 1 << 16;
#define ROT 1
#define HPRE 6

#define STAMP(k) do { } while (0)

__device__ __forceinline__ i32x4 make_srd(const void* p, u32 bytes) {
  u64 a = (u64)p;
  i32x4 r; r.x = (int)(u32)a; r.y = (int)(u32)(a >> 32) & 0xffff; r.z = (int)bytes; r.w = 0x00020000;
  return r;
}
#define BAR() asm volatile("s_barrier" ::: "memory")
#define BAR_L() asm volatile("s_waitcnt lgkmcnt(0)\n\ts_barrier" ::: "memory")
#define WAITV_BAR(n) asm volatile("s_waitcnt vmcnt(" #n ")\n\ts_barrier" ::: "memory")

#define DMA(v, s, o) "buffer_load_dwordx4 " v ", " s ", " o " offen lds\n\t"
#define STEP(o, ms, os) "s_add_u32 m0, m0, " #ms "\n\ts_add_u32 " o ", " o ", " #os "\n\t"
__device__ __forceinline__ void issue1(i32x4 sx, i32x4 sw, u32 vx, u32 vw, u32 ox, u32 ow, u32 mx, u32 mw) {
  asm volatile(
      "s_nop 4\n\t"
      "s_mov_b32 m0, %7\n\ts_nop 0\n\t"
      DMA("%3", "%5", "%1") STEP("%1", 1024, 36864) DMA("%3", "%5", "%1") STEP("%1", 3072, 110592)
      DMA("%3", "%5", "%1") STEP("%1", 1024, 36864) DMA("%3", "%5", "%1") STEP("%1", 3072, 110592)
      DMA("%3", "%5", "%1") STEP("%1", 1024, 36864) DMA("%3", "%5", "%1") STEP("%1", 3072, 110592)
      DMA("%3", "%5", "%1") STEP("%1", 1024, 36864) DMA("%3", "%5", "%1")
      "s_mov_b32 m0, %6\n\ts_nop 0\n\t"
      DMA("%2", "%4", "%0") STEP("%0", 2048, 18432) DMA("%2", "%4", "%0") STEP("%0", 2048, 18432)
      DMA("%2", "%4", "%0") STEP("%0", 2048, 18432) DMA("%2", "%4", "%0") STEP("%0", 2048, 18432)
      DMA("%2", "%4", "%0")
      : "+s"(ox), "+s"(ow) : "v"(vx), "v"(vw), "s"(sx), "s"(sw), "s"(mx), "s"(mw) : "memory", "scc");
}
__device__ __forceinline__ void issue2(i32x4 sw, u32 v, u32 o, u32 m) {
  asm volatile(
      "s_nop 4\n\t"
      "s_mov_b32 m0, %3\n\ts_nop 0\n\t"
      DMA("%1", "%2", "%0") STEP("%0", 2048, 18432) DMA("%1", "%2", "%0") STEP("%0", 2048, 18432)
      DMA("%1", "%2", "%0") STEP("%0", 2048, 18432) DMA("%1", "%2", "%0") STEP("%0", 2048, 18432)
      DMA("%1", "%2", "%0") STEP("%0", 2048, 18432) DMA("%1", "%2", "%0") STEP("%0", 2048, 18432)
      DMA("%1", "%2", "%0") STEP("%0", 2048, 18432) DMA("%1", "%2", "%0")
      : "+s"(o) : "v"(v), "s"(sw), "s"(m) : "memory", "scc");
}
__device__ __forceinline__ bf16x8 tr2(lptr p0, lptr p1) {
  s16x4 a = __builtin_amdgcn_ds_read_tr16_b64_v4i16((AS3 s16x4*)p0);
  s16x4 b = __builtin_amdgcn_ds_read_tr16_b64_v4i16((AS3 s16x4*)p1);
  s16x8 c = __builtin_shufflevector(a, b, 0, 1, 2, 3, 4, 5, 6, 7);
  return __builtin_bit_cast(bf16x8, c);
}
__device__ __forceinline__ u32 pk(float a, float b) {
  bf16x2 v = {(__bf16)a, (__bf16)b};
  return __builtin_bit_cast(u32, v);
}
__device__ __forceinline__ float lo(u32 p) { return __uint_as_float(p << 16); }
__device__ __forceinline__ float hi(u32 p) { return __uint_as_float(p & 0xffff0000u); }
__device__ __forceinline__ float relu(float v) {
  float r; asm("v_max_f32 %0, 0, %1" : "=v"(r) : "v"(v)); return r;
}
__device__ __forceinline__ u32 act2(float a0, float a1, f32x2 bv) {
  u32 p = pk(a0, a1);
  asm("" : "+v"(p));
  f32x2 v = {lo(p), hi(p)};
  v += bv;
  p = pk(v.x, v.y);
  asm("" : "+v"(p));
  f32x2 r = {relu(lo(p)), relu(hi(p))};
  r *= r;
  return pk(r.x, r.y);
}
#define MF(o, wv, h, c) "v_mfma_f32_16x16x32_bf16 " o ", " wv ", " h ", " c "\n\t"
#define CHK_A(n0, n1, n2, n3) "v_bitop3_b32 %[t0], " n0 ", " n1 ", %[S] bitop3:0x7e\n\tv_bitop3_b32 %[t1], " n2 ", " n3 ", %[S] bitop3:0x7e\n\t"
#define CHK_B "v_bitop3_b32 %[t0], %[t0], %[t1], %[M] bitop3:0xa8\n\tv_cmp_ne_u32 vcc, 0, %[t0]\n\t"
#define RETRY(n, n0, n1, n2, n3) \
  "s_cbranch_vccz 2f\n\t" \
  "1:\n\ts_cmp_eq_u32 %[cnt], 0\n\ts_cbranch_scc1 2f\n\ts_sub_u32 %[cnt], %[cnt], 1\n\t" \
  "buffer_load_dwordx4 " n ", %[vr], %[rw], %[sn] offen sc1\n\ts_waitcnt vmcnt(0)\n\t" \
  CHK_A(n0, n1, n2, n3) CHK_B "s_cbranch_vccnz 1b\n\t2:\n\t"
#define HR(a, b) "v[" #a ":" #b "]"
#define VR(a) "v" #a
#define STEP_MID(I, c0, c3, n0, n1, n2, n3, C0, C1, C2, C3, OC) \
  asm volatile(MF("%[o0]", "%[w0]", HR(c0, c3), C0) "s_waitcnt vmcnt(6)\n\t" MF("%[o1]", "%[w1]", HR(c0, c3), C1) \
      CHK_A(VR(n0), VR(n1), VR(n2), VR(n3)) MF("%[o2]", "%[w2]", HR(c0, c3), C2) CHK_B \
      RETRY(HR(n0, n3), VR(n0), VR(n1), VR(n2), VR(n3)) MF("%[o3]", "%[w3]", HR(c0, c3), C3) \
      "buffer_load_dwordx4 " HR(c0, c3) ", %[vn], %[rw], %[sc] offen sc1" \
      : [o0] OC(o[0]), [o1] OC(o[1]), [o2] OC(o[2]), [o3] OC(o[3]), [t0] "=&v"(t0), [t1] "=&v"(t1), [cnt] "+s"(cnt), \
        "+{" HR(c0, c3) "}"(H[I]), "+{" HR(n0, n3) "}"(H[I + 1]) \
      : [w0] "v"(W[I][0]), [w1] "v"(W[I][1]), [w2] "v"(W[I][2]), [w3] "v"(W[I][3]), [S] "s"(S), [M] "s"(0x80008000u), \
        [vr] "v"(vo), [vn] "v"(vn), [rw] "s"(sws), [sn] "s"(so[I + 1]), [sc] "s"(so[I]) \
      : "memory", "vcc", "scc")
#define ACC "%[o0]", "%[o1]", "%[o2]", "%[o3]", "+v"
#define STEP_X(...) STEP_MID(__VA_ARGS__)
#define HLOAD(I, c0, c3) \
  asm volatile("buffer_load_dwordx4 " HR(c0, c3) ", %1, %2, %3 offen sc1" : "={" HR(c0, c3) "}"(H[I]) : "v"(vo), "s"(sws), "s"(so[I]) : "memory")
__device__ __forceinline__ bool any_lane(bool c) { return __builtin_amdgcn_ballot_w64(c) != 0; }

__global__ void __launch_bounds__(512, 2)
ffn_kernel(const u16* __restrict__ x, const u16* __restrict__ w1, const u16* __restrict__ b1,
           const u16* __restrict__ w2, const u16* __restrict__ b2, u16* __restrict__ out, char* ws) {
  extern __shared__ __attribute__((aligned(16))) char lds_[];
  const lptr L = (lptr)lds_;
  const int b = blockIdx.x;
  const int s = (b & 7) >> 1, idx = (b & 1) * 32 + (b >> 3);
  if (idx >= 63) return;
  const int mb = idx / 9, g = idx - mb * 9;
  const int mt0 = mb < 4 ? 5 * mb : 4 * mb + 4, nm = mb < 4 ? 5 : 4;
  const int wave = __builtin_amdgcn_readfirstlane(threadIdx.x >> 6), lane = threadIdx.x & 63;
  STAMP(0);
  asm volatile("" :: "s"(x), "s"(w1), "s"(w2), "s"(ws), "s"(b1));
  const __amdgpu_buffer_rsrc_t rw = __builtin_amdgcn_make_buffer_rsrc(ws, 0, WS_END, 0x00020000);
  const int q = lane >> 4;

  if (wave >= 4) {
    const int u = wave - 4;
    if (u < 2) {
      __builtin_amdgcn_s_setprio(3);
      const i32x4 sx = make_srd(x, (u32)(mt0 + nm) * 16u * 1152u), sw1 = make_srd(w1, 576u * 9216u), sw2 = make_srd(w2, 4608u * 1152u);
      const u32 par = (u32)u;
      const u32 vx = (u32)(lane >> 3) * 1152u + (u32)((lane & 7) ^ (4 * par + (lane >> 4))) * 16u;
      const u32 vw = (u32)(lane >> 4) * 9216u + (u32)(((lane & 15) >> 1) ^ ((lane >> 4) | (par << 2))) * 32u + (u32)(lane & 1) * 16u;
      const u32 v2 = (u32)(lane >> 3) * 1152u + (u32)(((lane & 7) >> 1) ^ ((lane >> 4) & 3)) * 32u + (u32)(lane & 1) * 16u;
      const u32 oxb = (u32)(mt0 * 16 + 8 * par) * 1152u, owb = par * 8u * 9216u + (u32)(s * 9 + g) * 256u;
      const u32 o2b = (u32)(s * 1152 + 8 * par) * 1152u + (u32)g * 128u;
      const u32 mxb = par * 1024u, mwb = 10240u + par * 2048u;
      u32 kp = ROT ? (u32)(mb * 4 / 3) : 0u, slot = 0;
      auto st1 = [&]() {
        issue1(sx, sw1, vx, vw, oxb + kp * 128u, owb + kp * (64u * 9216u), mxb + slot, mwb + slot);
        kp = kp == 8u ? 0u : kp + 1u; slot = slot == 5u * SLOT ? 0u : slot + SLOT;
      };
      u32 gp = (u32)g;
      auto st2 = [&]() {
        issue2(sw2, v2, o2b + gp * (128u * 1152u), mxb + slot);
        gp = gp == 8u ? 0u : gp + 1u; slot = slot == 5u * SLOT ? 0u : slot + SLOT;
      };
      st1(); st1();
      STAMP(1);
#pragma unroll 1
      for (int j = 0; j < 7; ++j) { WAITV_BAR(13); st1(); }
      WAITV_BAR(13); st2(); st2();
      STAMP(2);
#pragma unroll 1
      for (int j = 8; j < 15; ++j) { WAITV_BAR(16); st2(); }
      WAITV_BAR(16); WAITV_BAR(8); WAITV_BAR(0);
      STAMP(3);
      __builtin_amdgcn_s_setprio(0);
    } else {
      const u32 Ev = __builtin_amdgcn_raw_buffer_load_b32(rw, 0, mb * 256, 16);
      if (lane < 20) *(AS3 u32*)(L + LDS_FLAGS + lane * 4) = 0u;
      BAR_L();
#pragma unroll 1
      for (int j = 1; j < 7; ++j) BAR();
      *(AS3 u32*)(L + LDS_FLAGS + 128) = ~Ev;
      BAR_L();
#pragma unroll 1
      for (int j = 8; j < 18; ++j) BAR();
    }
    const u32 T = __builtin_amdgcn_readfirstlane(*(AS3 u32*)(L + LDS_FLAGS + 128));
    const u32 sgn = (T << 16) & 0x80000000u, nib = T & 15u;
    u32x2 bb = {0u, 0u};
    if (u == s) bb = *(const u32x2*)(b2 + g * 64 + s * 16 + q * 4);
    const u32 pout = WS_P + (u32)(((mb * 9 + g) * 4 + u) * 4 + s) * 5120u;
#pragma unroll 1
    for (int mt = 0; mt < nm; ++mt) {
#pragma unroll 1
      for (int it = 0; it < (SPIN_MAX << 6); ++it) {
        const u32x4 f = *(volatile AS3 u32x4*)(L + LDS_FLAGS + mt * 16);
        if (__builtin_amdgcn_readfirstlane(f.x & f.y & f.z & f.w) == 1u) break;
        __builtin_amdgcn_s_sleep(1);
      }
      asm volatile("" ::: "memory");
      const lptr r = L + mt * SLOT + u * 1024 + lane * 16;
      f32x4 v = *(AS3 f32x4*)r;
      v += *(AS3 f32x4*)(r + 4096); v += *(AS3 f32x4*)(r + 8192); v += *(AS3 f32x4*)(r + 12288);
      u32x4 w = __builtin_bit_cast(u32x4, v) ^ sgn;
      if (u == s) *(AS3 u32x4*)r = w;
      else __builtin_amdgcn_raw_buffer_store_b128((w & ~15u) | nib, rw, lane * 16, pout + mt * 1024, 16);
      STAMP(4 + mt);
    }
    if (u == s) {
      const u32 pin = WS_P + (u32)((mb * 9 + g) * 4 + s) * 4 * 5120u;
      u32 so[3];
#pragma unroll
      for (int k = 0; k < 3; ++k) so[k] = pin + (u32)(k + (k >= s ? 1 : 0)) * 5120u;
      u32x4 p[5][3];
#pragma unroll
      for (int mt = 0; mt < 5; ++mt) {
        const u32 vo = (mt < 4 || nm == 5) ? (u32)lane * 16u + mt * 1024u : 0x80000000u;
#pragma unroll
        for (int k = 0; k < 3; ++k) p[mt][k] = __builtin_amdgcn_raw_buffer_load_b128(rw, vo, so[k], 16);
      }
#pragma unroll
      for (int mt = 0; mt < 5; ++mt) {
        if (mt < nm) {
#pragma unroll 1
          for (int it = 0; it < SPIN_MAX; ++it) {
            u32 bad = 0;
#pragma unroll
            for (int k = 0; k < 3; ++k) bad |= (p[mt][k].x ^ nib) | (p[mt][k].y ^ nib) | (p[mt][k].z ^ nib) | (p[mt][k].w ^ nib);
            if (!any_lane((bad & 15u) != 0u)) break;
            asm volatile("" ::: "memory");
#pragma unroll
            for (int k = 0; k < 3; ++k) p[mt][k] = __builtin_amdgcn_raw_buffer_load_b128(rw, lane * 16 + mt * 1024, so[k], 16);
          }
          f32x4 v = *(AS3 f32x4*)(L + mt * SLOT + u * 1024 + lane * 16);
#pragma unroll
          for (int k = 0; k < 3; ++k) v += __builtin_bit_cast(f32x4, p[mt][k] & ~15u);
          const u32 p0 = pk(v.x, v.y), p1 = pk(v.z, v.w);
          u32x2 o;
          o.x = pk(lo(p0) + lo(bb.x), hi(p0) + hi(bb.x));
          o.y = pk(lo(p1) + lo(bb.y), hi(p1) + hi(bb.y));
          *(u32x2*)(out + (size_t)((mt0 + mt) * 16 + (lane & 15)) * 576 + g * 64 + s * 16 + q * 4) = o;
        }
      }
      if (s == 0 && g == 0 && lane == 0) __builtin_amdgcn_raw_buffer_store_b32(T, rw, 0, mb * 256, 16);
      STAMP(10);
    }
    return;
  }

  const int w = wave;
  const int a = (lane >> 2) & 3, p = lane & 3, m = lane & 15;
  const u32x2 bA = *(const u32x2*)(b1 + (s * 9 + g) * 128 + w * 32 + q * 4);
  const u32x2 bB = *(const u32x2*)(b1 + (s * 9 + g) * 128 + w * 32 + 16 + q * 4);
  const u32 Ev = __builtin_amdgcn_raw_buffer_load_b32(rw, 0, mb * 256, 16);
  f32x4 acc[2][5];
#pragma unroll
  for (int t = 0; t < 2; ++t)
#pragma unroll
    for (int mt = 0; mt < 5; ++mt) acc[t][mt] = (f32x4){0.f, 0.f, 0.f, 0.f};
  {
    const u32 xa0 = (u32)m * 128u + (u32)(q ^ (m >> 1)) * 16u;
    const u32 wa0 = 10240u + (u32)(8 * q + a) * 256u + (u32)((2 * w) ^ (a | ((q & 1) << 2))) * 32u + (u32)p * 8u;
    u32 slot = 0;
#pragma unroll 1
    for (int j = 0; j < 9; ++j) {
      BAR();
      bf16x8 A[2][2], B[2][5];
#pragma unroll
      for (int ks = 0; ks < 2; ++ks) {
#pragma unroll
        for (int t = 0; t < 2; ++t) {
          const lptr pw = L + (slot + (wa0 ^ (t * 32u))) + ks * 8192;
          A[ks][t] = tr2(pw, pw + 1024);
        }
#pragma unroll
        for (int mt = 0; mt < 5; ++mt) B[ks][mt] = *(AS3 bf16x8*)(L + (slot + (xa0 ^ (ks * 64u))) + mt * 2048);
        __builtin_amdgcn_sched_barrier(0);
      }
#pragma unroll
      for (int ks = 0; ks < 2; ++ks)
#pragma unroll
        for (int mt = 0; mt < 5; ++mt)
#pragma unroll
          for (int t = 0; t < 2; ++t) acc[t][mt] = __builtin_amdgcn_mfma_f32_16x16x32_bf16(A[ks][t], B[ks][mt], acc[t][mt], 0, 0, 0);
      slot = slot == 5u * SLOT ? 0u : slot + SLOT;
    }
  }
  STAMP(1);
  const u32 T = ~__builtin_amdgcn_readfirstlane(Ev);
  const i32x4 sws = make_srd(ws, WS_END);
  const f32x2 bv[4] = {{lo(bA.x), hi(bA.x)}, {lo(bA.y), hi(bA.y)}, {lo(bB.x), hi(bB.x)}, {lo(bB.y), hi(bB.y)}};
  u32 vo = (u32)lane * 16u;
  const u32 S = ((T >> 15) & 1u) * 0x80008000u;
  const u32 hb = WS_H + (u32)(mb * 4 + s) * (5u * 36864u) + (u32)w * 1024u;
  u32 so[9];
#pragma unroll
  for (int i = 0; i < 9; ++i) so[i] = hb + (u32)(g + i >= 9 ? g + i - 9 : g + i) * 4096u;
  bf16x8 W[9][4];
  u32x4 H[9];
  const u32 wb = (u32)(32 * w + 4 * q + a) * 128u + (u32)((2 * q + (a >> 1)) & 3) * 32u + (u32)p * 8u;
#pragma unroll
  for (int i = 0; i < 9; ++i) {
    if (i < 5) {
      u32x4 h;
      h.x = act2(acc[0][i].x, acc[0][i].y, bv[0]) | S; h.y = act2(acc[0][i].z, acc[0][i].w, bv[1]) | S;
      h.z = act2(acc[1][i].x, acc[1][i].y, bv[2]) | S; h.w = act2(acc[1][i].z, acc[1][i].w, bv[3]) | S;
      if (i < 4 || nm == 5) __builtin_amdgcn_raw_buffer_store_b128(h, rw, lane * 16, so[0] + i * 36864u, 16);
      const int k = i * 4 + w;
      *(AS3 u32x4*)(L + (3 * SLOT + 16384 + k * 1024 + (k >= 10 ? 16384 : 0)) + lane * 16) = h;
      if (i == 4) STAMP(2);
    }
    BAR_L();
#pragma unroll
    for (int t = 0; t < 4; ++t) {
      const lptr pw = L + (wb ^ (t * 32u)) + ((9 + i) % 6) * SLOT;
      W[i][t] = tr2(pw, pw + 2048);
    }
    if (i == HPRE) {
      asm volatile("s_nop 4");
      HLOAD(1, 224, 227); HLOAD(2, 228, 231); HLOAD(3, 232, 235); HLOAD(4, 236, 239);
      HLOAD(5, 240, 243); HLOAD(6, 244, 247); HLOAD(7, 248, 251); HLOAD(8, 252, 255);
    }
    __builtin_amdgcn_sched_barrier(0);
  }
  STAMP(3);
  u32 cnt = SPIN_MAX, t0, t1;
  asm volatile("s_waitcnt vmcnt(7)\n\t" CHK_A("v224", "v225", "v226", "v227") CHK_B RETRY("v[224:227]", "v224", "v225", "v226", "v227")
               : [t0] "=&v"(t0), [t1] "=&v"(t1), [cnt] "+s"(cnt), "+{v[224:227]}"(H[1])
               : [S] "s"(S), [M] "s"(0x80008000u), [vr] "v"(vo), [rw] "s"(sws), [sn] "s"(so[1]) : "memory", "vcc", "scc");
#pragma unroll 1
  for (int mt = 0; mt < nm; ++mt) {
    const int k = mt * 4 + w;
    H[0] = *(AS3 u32x4*)(L + (3 * SLOT + 16384 + k * 1024 + (k >= 10 ? 16384 : 0)) + lane * 16);
    const u32 vn = mt + 1 < nm ? vo + 36864u : 0x80000000u;
    const u32 mn = mt + 1 < nm ? 0x80008000u : 0u;
    f32x4 o[4];
    STEP_X(1, 224, 227, 228, 229, 230, 231, "0", "0", "0", "0", "=&v");
    STEP_X(2, 228, 231, 232, 233, 234, 235, ACC);
    STEP_X(3, 232, 235, 236, 237, 238, 239, ACC);
    STEP_X(4, 236, 239, 240, 241, 242, 243, ACC);
    STEP_X(5, 240, 243, 244, 245, 246, 247, ACC);
    STEP_X(6, 244, 247, 248, 249, 250, 251, ACC);
    STEP_X(7, 248, 251, 252, 253, 254, 255, ACC);
    asm volatile(MF("%[o0]", "%[w0]", "v[252:255]", "%[o0]") MF("%[o1]", "%[w1]", "v[252:255]", "%[o1]")
                 MF("%[o2]", "%[w2]", "v[252:255]", "%[o2]") MF("%[o3]", "%[w3]", "v[252:255]", "%[o3]")
                 "buffer_load_dwordx4 v[252:255], %[vn], %[rw], %[sc] offen sc1"
                 : [o0] "+v"(o[0]), [o1] "+v"(o[1]), [o2] "+v"(o[2]), [o3] "+v"(o[3]), "+{v[252:255]}"(H[8])
                 : [w0] "v"(W[8][0]), [w1] "v"(W[8][1]), [w2] "v"(W[8][2]), [w3] "v"(W[8][3]), [vn] "v"(vn), [rw] "s"(sws), [sc] "s"(so[8])
                 : "memory");
    asm volatile(MF("%[o0]", "%[w0]", "%[h]", "%[o0]") "s_waitcnt vmcnt(7)\n\t" MF("%[o1]", "%[w1]", "%[h]", "%[o1]")
                 CHK_A("v224", "v225", "v226", "v227") MF("%[o2]", "%[w2]", "%[h]", "%[o2]") CHK_B
                 RETRY("v[224:227]", "v224", "v225", "v226", "v227") MF("%[o3]", "%[w3]", "%[h]", "%[o3]") "s_nop 7"
                 : [o0] "+v"(o[0]), [o1] "+v"(o[1]), [o2] "+v"(o[2]), [o3] "+v"(o[3]), [t0] "=&v"(t0), [t1] "=&v"(t1),
                   [cnt] "+s"(cnt), "+{v[224:227]}"(H[1])
                 : [w0] "v"(W[0][0]), [w1] "v"(W[0][1]), [w2] "v"(W[0][2]), [w3] "v"(W[0][3]), [h] "v"(H[0]), [S] "s"(S),
                   [M] "s"(mn), [vr] "v"(vn), [rw] "s"(sws), [sn] "s"(so[1])
                 : "memory", "vcc", "scc");
    const lptr r = L + mt * SLOT + w * 4096 + lane * 16;
#pragma unroll
    for (int t = 0; t < 4; ++t) *(AS3 f32x4*)(r + t * 1024) = o[t];
    asm volatile("" ::: "memory");
    if (lane == 0) *(volatile AS3 u32*)(L + LDS_FLAGS + mt * 16 + w * 4) = 1u;
    vo = vn;
    STAMP(4 + mt);
  }
}

extern "C" void kernel_launch(void* const* d_in, const int* in_sizes, int n_in, void* d_out, int out_size, void* d_ws,
                              size_t ws_size, hipStream_t stream) {
  (void)in_sizes; (void)n_in; (void)out_size; (void)ws_size;
  (void)hipFuncSetAttribute((const void*)ffn_kernel, hipFuncAttributeMaxDynamicSharedMemorySize, LDS_BYTES);
  ffn_kernel<<<256, 512, LDS_BYTES, stream>>>((const u16*)d_in[0], (const u16*)d_in[1], (const u16*)d_in[2],
                                              (const u16*)d_in[3], (const u16*)d_in[4], (u16*)d_out, (char*)d_ws);
}
```

```cpp
#include <hip/hip_runtime.h>
#include <cstdio>
#define PROF 1

typedef unsigned short u16;
typedef unsigned int u32;
typedef unsigned long long u64;
typedef __bf16 bf16x8 __attribute__((ext_vector_type(8)));
typedef __bf16 bf16x2 __attribute__((ext_vector_type(2)));
typedef short s16x4 __attribute__((ext_vector_type(4)));
typedef short s16x8 __attribute__((ext_vector_type(8)));
typedef float f32x4 __attribute__((ext_vector_type(4)));
typedef float f32x2 __attribute__((ext_vector_type(2)));
typedef u32 u32x4 __attribute__((ext_vector_type(4)));
typedef u32 u32x2 __attribute__((ext_vector_type(2)));
typedef int i32x4 __attribute__((ext_vector_type(4)));
#define AS3 __attribute__((address_space(3)))
#define AS4 __attribute__((address_space(4)))
typedef AS3 char* lptr;

constexpr int SLOT = 26 * 1024;
constexpr int LDS_FLAGS = 6 * SLOT;
constexpr int LDS_BYTES = LDS_FLAGS + 256;
constexpr u32 WS_H = 4096;
constexpr u32 WS_P = WS_H + 7u * 4 * 5 * 36 * 1024;
constexpr u32 WS_END = WS_P + 7u * 9 * 16 * 5 * 1024;
constexpr int SPIN_MAX = 1 << 16;
#define ROT 1
#define HPRE 6

#define STAMP(k) asm volatile("s_memrealtime %0\n\ts_waitcnt lgkmcnt(0)" : "=s"(ts[k]) :: "memory")
#define STAMPR(k) asm volatile("s_memrealtime %0\n\ts_waitcnt lgkmcnt(0)" : "=s"(ts[k]) :: "memory")
#define FLUSH() do { STAMPR(13); if (lane == 0) { _Pragma("unroll") for (int k_ = 0; k_ < 14; ++k_) ((u64*)(ws + WS_END))[(blockIdx.x * 8 + wave) * 32 + k_] = ts[k_]; } } while (0)

__device__ __forceinline__ i32x4 make_srd(const void* p, u32 bytes) {
  u64 a = (u64)p;
  i32x4 r; r.x = (int)(u32)a; r.y = (int)(u32)(a >> 32) & 0xffff; r.z = (int)bytes; r.w = 0x00020000;
  return r;
}
#define BAR() asm volatile("s_barrier" ::: "memory")
#define BAR_L() asm volatile("s_waitcnt lgkmcnt(0)\n\ts_barrier" ::: "memory")
#define WAITV_BAR(n) asm volatile("s_waitcnt vmcnt(" #n ")\n\ts_barrier" ::: "memory")

#define DMA(v, s, o) "buffer_load_dwordx4 " v ", " s ", " o " offen lds\n\t"
#define STEP(o, ms, os) "s_add_u32 m0, m0, " #ms "\n\ts_add_u32 " o ", " o ", " #os "\n\t"
__device__ __forceinline__ void issue1(i32x4 sx, i32x4 sw, u32 vx, u32 vw, u32 ox, u32 ow, u32 mx, u32 mw) {
  asm volatile(
      "s_nop 4\n\t"
      "s_mov_b32 m0, %7\n\ts_nop 0\n\t"
      DMA("%3", "%5", "%1") STEP("%1", 1024, 36864) DMA("%3", "%5", "%1") STEP("%1", 3072, 110592)
      DMA("%3", "%5", "%1") STEP("%1", 1024, 36864) DMA("%3", "%5", "%1") STEP("%1", 3072, 110592)
      DMA("%3", "%5", "%1") STEP("%1", 1024, 36864) DMA("%3", "%5", "%1") STEP("%1", 3072, 110592)
      DMA("%3", "%5", "%1") STEP("%1", 1024, 36864) DMA("%3", "%5", "%1")
      "s_mov_b32 m0, %6\n\ts_nop 0\n\t"
      DMA("%2", "%4", "%0") STEP("%0", 2048, 18432) DMA("%2", "%4", "%0") STEP("%0", 2048, 18432)
      DMA("%2", "%4", "%0") STEP("%0", 2048, 18432) DMA("%2", "%4", "%0") STEP("%0", 2048, 18432)
      DMA("%2", "%4", "%0")
      : "+s"(ox), "+s"(ow) : "v"(vx), "v"(vw), "s"(sx), "s"(sw), "s"(mx), "s"(mw) : "memory", "scc");
}
__device__ __forceinline__ void issue2(i32x4 sw, u32 v, u32 o, u32 m) {
  asm volatile(
      "s_nop 4\n\t"
      "s_mov_b32 m0, %3\n\ts_nop 0\n\t"
      DMA("%1", "%2", "%0") STEP("%0", 2048, 18432) DMA("%1", "%2", "%0") STEP("%0", 2048, 18432)
      DMA("%1", "%2", "%0") STEP("%0", 2048, 18432) DMA("%1", "%2", "%0") STEP("%0", 2048, 18432)
      DMA("%1", "%2", "%0") STEP("%0", 2048, 18432) DMA("%1", "%2", "%0") STEP("%0", 2048, 18432)
      DMA("%1", "%2", "%0") STEP("%0", 2048, 18432) DMA("%1", "%2", "%0")
      : "+s"(o) : "v"(v), "s"(sw), "s"(m) : "memory", "scc");
}
__device__ __forceinline__ bf16x8 tr2(lptr p0, lptr p1) {
  s16x4 a = __builtin_amdgcn_ds_read_tr16_b64_v4i16((AS3 s16x4*)p0);
  s16x4 b = __builtin_amdgcn_ds_read_tr16_b64_v4i16((AS3 s16x4*)p1);
  s16x8 c = __builtin_shufflevector(a, b, 0, 1, 2, 3, 4, 5, 6, 7);
  return __builtin_bit_cast(bf16x8, c);
}
__device__ __forceinline__ u32 pk(float a, float b) {
  bf16x2 v = {(__bf16)a, (__bf16)b};
  return __builtin_bit_cast(u32, v);
}
__device__ __forceinline__ float lo(u32 p) { return __uint_as_float(p << 16); }
__device__ __forceinline__ float hi(u32 p) { return __uint_as_float(p & 0xffff0000u); }
__device__ __forceinline__ float relu(float v) {
  float r; asm("v_max_f32 %0, 0, %1" : "=v"(r) : "v"(v)); return r;
}
__device__ __forceinline__ u32 act2(float a0, float a1, f32x2 bv) {
  u32 p = pk(a0, a1);
  asm("" : "+v"(p));
  f32x2 v = {lo(p), hi(p)};
  v += bv;
  p = pk(v.x, v.y);
  asm("" : "+v"(p));
  f32x2 r = {relu(lo(p)), relu(hi(p))};
  r *= r;
  return pk(r.x, r.y);
}
#define MF(o, wv, h, c) "v_mfma_f32_16x16x32_bf16 " o ", " wv ", " h ", " c "\n\t"
#define CHK_A(n0, n1, n2, n3) "v_bitop3_b32 %[t0], " n0 ", " n1 ", %[S] bitop3:0x7e\n\tv_bitop3_b32 %[t1], " n2 ", " n3 ", %[S] bitop3:0x7e\n\t"
#define CHK_B "v_bitop3_b32 %[t0], %[t0], %[t1], %[M] bitop3:0xa8\n\tv_cmp_ne_u32 vcc, 0, %[t0]\n\t"
#define RETRY(n, n0, n1, n2, n3) \
  "s_cbranch_vccz 2f\n\t" \
  "1:\n\ts_cmp_eq_u32 %[cnt], 0\n\ts_cbranch_scc1 2f\n\ts_sub_u32 %[cnt], %[cnt], 1\n\t" \
  "buffer_load_dwordx4 " n ", %[vr], %[rw], %[sn] offen sc1\n\ts_waitcnt vmcnt(0)\n\t" \
  CHK_A(n0, n1, n2, n3) CHK_B "s_cbranch_vccnz 1b\n\t2:\n\t"
#define HR(a, b) "v[" #a ":" #b "]"
#define VR(a) "v" #a
#define STEP_MID(I, c0, c3, n0, n1, n2, n3, C0, C1, C2, C3, OC) \
  asm volatile(MF("%[o0]", "%[w0]", HR(c0, c3), C0) "s_waitcnt vmcnt(6)\n\t" MF("%[o1]", "%[w1]", HR(c0, c3), C1) \
      CHK_A(VR(n0), VR(n1), VR(n2), VR(n3)) MF("%[o2]", "%[w2]", HR(c0, c3), C2) CHK_B \
      RETRY(HR(n0, n3), VR(n0), VR(n1), VR(n2), VR(n3)) MF("%[o3]", "%[w3]", HR(c0, c3), C3) \
      "buffer_load_dwordx4 " HR(c0, c3) ", %[vn], %[rw], %[sc] offen sc1" \
      : [o0] OC(o[0]), [o1] OC(o[1]), [o2] OC(o[2]), [o3] OC(o[3]), [t0] "=&v"(t0), [t1] "=&v"(t1), [cnt] "+s"(cnt), \
        "+{" HR(c0, c3) "}"(H[I]), "+{" HR(n0, n3) "}"(H[I + 1]) \
      : [w0] "v"(W[I][0]), [w1] "v"(W[I][1]), [w2] "v"(W[I][2]), [w3] "v"(W[I][3]), [S] "s"(S), [M] "s"(0x80008000u), \
        [vr] "v"(vo), [vn] "v"(vn), [rw] "s"(sws), [sn] "s"(so[I + 1]), [sc] "s"(so[I]) \
      : "memory", "vcc", "scc")
#define ACC "%[o0]", "%[o1]", "%[o2]", "%[o3]", "+v"
#define STEP_X(...) STEP_MID(__VA_ARGS__)
#define HLOAD(I, c0, c3) \
  asm volatile("buffer_load_dwordx4 " HR(c0, c3) ", %1, %2, %3 offen sc1" : "={" HR(c0, c3) "}"(H[I]) : "v"(vo), "s"(sws), "s"(so[I]) : "memory")
__device__ __forceinline__ bool any_lane(bool c) { return __builtin_amdgcn_ballot_w64(c) != 0; }

__global__ void __launch_bounds__(512, 2)
ffn_kernel(const u16* __restrict__ x, const u16* __restrict__ w1, const u16* __restrict__ b1,
           const u16* __restrict__ w2, const u16* __restrict__ b2, u16* __restrict__ out, char* ws) {
  extern __shared__ __attribute__((aligned(16))) char lds_[];
  const lptr L = (lptr)lds_;
  const int b = blockIdx.x;
  const int s = (b & 7) >> 1, idx = (b & 1) * 32 + (b >> 3);
  if (idx >= 63) return;
  const int mb = idx / 9, g = idx - mb * 9;
  const int mt0 = mb < 4 ? 5 * mb : 4 * mb + 4, nm = mb < 4 ? 5 : 4;
  const int wave = __builtin_amdgcn_readfirstlane(threadIdx.x >> 6), lane = threadIdx.x & 63;
  u64 ts[14] = {0, 0, 0, 0, 0, 0, 0, 0, 0, 0, 0, 0, 0, 0};
  STAMPR(12);
  STAMP(0);
  asm volatile("" :: "s"(x), "s"(w1), "s"(w2), "s"(ws), "s"(b1));
  const __amdgpu_buffer_rsrc_t rw = __builtin_amdgcn_make_buffer_rsrc(ws, 0, WS_END, 0x00020000);
  const int q = lane >> 4;

  if (wave >= 4) {
    const int u = wave - 4;
    if (u < 2) {
      __builtin_amdgcn_s_setprio(3);
      const i32x4 sx = make_srd(x, (u32)(mt0 + nm) * 16u * 1152u), sw1 = make_srd(w1, 576u * 9216u), sw2 = make_srd(w2, 4608u * 1152u);
      const u32 par = (u32)u;
      const u32 vx = (u32)(lane >> 3) * 1152u + (u32)((lane & 7) ^ (4 * par + (lane >> 4))) * 16u;
      const u32 vw = (u32)(lane >> 4) * 9216u + (u32)(((lane & 15) >> 1) ^ ((lane >> 4) | (par << 2))) * 32u + (u32)(lane & 1) * 16u;
      const u32 v2 = (u32)(lane >> 3) * 1152u + (u32)(((lane & 7) >> 1) ^ ((lane >> 4) & 3)) * 32u + (u32)(lane & 1) * 16u;
      const u32 oxb = (u32)(mt0 * 16 + 8 * par) * 1152u, owb = par * 8u * 9216u + (u32)(s * 9 + g) * 256u;
      const u32 o2b = (u32)(s * 1152 + 8 * par) * 1152u + (u32)g * 128u;
      const u32 mxb = par * 1024u, mwb = 10240u + par * 2048u;
      u32 kp = ROT ? (u32)(mb * 4 / 3) : 0u, slot = 0;
      auto st1 = [&]() {
        issue1(sx, sw1, vx, vw, oxb + kp * 128u, owb + kp * (64u * 9216u), mxb + slot, mwb + slot);
        kp = kp == 8u ? 0u : kp + 1u; slot = slot == 5u * SLOT ? 0u : slot + SLOT;
      };
      u32 gp = (u32)g;
      auto st2 = [&]() {
        issue2(sw2, v2, o2b + gp * (128u * 1152u), mxb + slot);
        gp = gp == 8u ? 0u : gp + 1u; slot = slot == 5u * SLOT ? 0u : slot + SLOT;
      };
      st1(); st1();
      STAMP(1);
#pragma unroll 1
      for (int j = 0; j < 7; ++j) { WAITV_BAR(13); st1(); }
      WAITV_BAR(13); st2(); st2();
      STAMP(2);
#pragma unroll 1
      for (int j = 8; j < 15; ++j) { WAITV_BAR(16); st2(); }
      WAITV_BAR(16); WAITV_BAR(8); WAITV_BAR(0);
      STAMP(3);
      __builtin_amdgcn_s_setprio(0);
    } else {
      const u32 Ev = __builtin_amdgcn_raw_buffer_load_b32(rw, 0, mb * 256, 16);
      if (lane < 20) *(AS3 u32*)(L + LDS_FLAGS + lane * 4) = 0u;
      BAR_L();
#pragma unroll 1
      for (int j = 1; j < 7; ++j) BAR();
      *(AS3 u32*)(L + LDS_FLAGS + 128) = ~Ev;
      BAR_L();
#pragma unroll 1
      for (int j = 8; j < 18; ++j) BAR();
    }
    const u32 T = __builtin_amdgcn_readfirstlane(*(AS3 u32*)(L + LDS_FLAGS + 128));
    const u32 sgn = (T << 16) & 0x80000000u, nib = T & 15u;
    u32x2 bb = {0u, 0u};
    if (u == s) bb = *(const u32x2*)(b2 + g * 64 + s * 16 + q * 4);
    const u32 pout = WS_P + (u32)(((mb * 9 + g) * 4 + u) * 4 + s) * 5120u;
#pragma unroll 1
    for (int mt = 0; mt < nm; ++mt) {
#pragma unroll 1
      for (int it = 0; it < (SPIN_MAX << 6); ++it) {
        const u32x4 f = *(volatile AS3 u32x4*)(L + LDS_FLAGS + mt * 16);
        if (__builtin_amdgcn_readfirstlane(f.x & f.y & f.z & f.w) == 1u) break;
        __builtin_amdgcn_s_sleep(1);
      }
      asm volatile("" ::: "memory");
      const lptr r = L + mt * SLOT + u * 1024 + lane * 16;
      f32x4 v = *(AS3 f32x4*)r;
      v += *(AS3 f32x4*)(r + 4096); v += *(AS3 f32x4*)(r + 8192); v += *(AS3 f32x4*)(r + 12288);
      u32x4 w = __builtin_bit_cast(u32x4, v) ^ sgn;
      if (u == s) *(AS3 u32x4*)r = w;
      else __builtin_amdgcn_raw_buffer_store_b128((w & ~15u) | nib, rw, lane * 16, pout + mt * 1024, 16);
      if (mt == 0) STAMP(4);
    }
    STAMP(8);
    if (u == s) {
      const u32 pin = WS_P + (u32)((mb * 9 + g) * 4 + s) * 4 * 5120u;
      u32 so[3];
#pragma unroll
      for (int k = 0; k < 3; ++k) so[k] = pin + (u32)(k + (k >= s ? 1 : 0)) * 5120u;
      u32x4 p[5][3];
#pragma unroll
      for (int mt = 0; mt < 5; ++mt) {
        const u32 vo = (mt < 4 || nm == 5) ? (u32)lane * 16u + mt * 1024u : 0x80000000u;
#pragma unroll
        for (int k = 0; k < 3; ++k) p[mt][k] = __builtin_amdgcn_raw_buffer_load_b128(rw, vo, so[k], 16);
      }
#pragma unroll
      for (int mt = 0; mt < 5; ++mt) {
        if (mt < nm) {
#pragma unroll 1
          for (int it = 0; it < SPIN_MAX; ++it) {
            u32 bad = 0;
#pragma unroll
            for (int k = 0; k < 3; ++k) bad |= (p[mt][k].x ^ nib) | (p[mt][k].y ^ nib) | (p[mt][k].z ^ nib) | (p[mt][k].w ^ nib);
            if (!any_lane((bad & 15u) != 0u)) break;
            asm volatile("" ::: "memory");
#pragma unroll
            for (int k = 0; k < 3; ++k) p[mt][k] = __builtin_amdgcn_raw_buffer_load_b128(rw, lane * 16 + mt * 1024, so[k], 16);
          }
          f32x4 v = *(AS3 f32x4*)(L + mt * SLOT + u * 1024 + lane * 16);
#pragma unroll
          for (int k = 0; k < 3; ++k) v += __builtin_bit_cast(f32x4, p[mt][k] & ~15u);
          const u32 p0 = pk(v.x, v.y), p1 = pk(v.z, v.w);
          u32x2 o;
          o.x = pk(lo(p0) + lo(bb.x), hi(p0) + hi(bb.x));
          o.y = pk(lo(p1) + lo(bb.y), hi(p1) + hi(bb.y));
          *(u32x2*)(out + (size_t)((mt0 + mt) * 16 + (lane & 15)) * 576 + g * 64 + s * 16 + q * 4) = o;
        }
      }
      if (s == 0 && g == 0 && lane == 0) __builtin_amdgcn_raw_buffer_store_b32(T, rw, 0, mb * 256, 16);
      STAMP(10);
    }
    FLUSH();
    return;
  }

  const int w = wave;
  const int a = (lane >> 2) & 3, p = lane & 3, m = lane & 15;
  const u32x2 bA = *(const u32x2*)(b1 + (s * 9 + g) * 128 + w * 32 + q * 4);
  const u32x2 bB = *(const u32x2*)(b1 + (s * 9 + g) * 128 + w * 32 + 16 + q * 4);
  const u32 Ev = __builtin_amdgcn_raw_buffer_load_b32(rw, 0, mb * 256, 16);
  f32x4 acc[2][5];
#pragma unroll
  for (int t = 0; t < 2; ++t)
#pragma unroll
    for (int mt = 0; mt < 5; ++mt) acc[t][mt] = (f32x4){0.f, 0.f, 0.f, 0.f};
  {
    const u32 xa0 = (u32)m * 128u + (u32)(q ^ (m >> 1)) * 16u;
    const u32 wa0 = 10240u + (u32)(8 * q + a) * 256u + (u32)((2 * w) ^ (a | ((q & 1) << 2))) * 32u + (u32)p * 8u;
    u32 slot = 0;
#pragma unroll 1
    for (int j = 0; j < 9; ++j) {
      BAR();
      bf16x8 A[2][2], B[2][5];
#pragma unroll
      for (int ks = 0; ks < 2; ++ks) {
#pragma unroll
        for (int t = 0; t < 2; ++t) {
          const lptr pw = L + (slot + (wa0 ^ (t * 32u))) + ks * 8192;
          A[ks][t] = tr2(pw, pw + 1024);
        }
#pragma unroll
        for (int mt = 0; mt < 5; ++mt) B[ks][mt] = *(AS3 bf16x8*)(L + (slot + (xa0 ^ (ks * 64u))) + mt * 2048);
        __builtin_amdgcn_sched_barrier(0);
      }
#pragma unroll
      for (int ks = 0; ks < 2; ++ks)
#pragma unroll
        for (int mt = 0; mt < 5; ++mt)
#pragma unroll
          for (int t = 0; t < 2; ++t) acc[t][mt] = __builtin_amdgcn_mfma_f32_16x16x32_bf16(A[ks][t], B[ks][mt], acc[t][mt], 0, 0, 0);
      slot = slot == 5u * SLOT ? 0u : slot + SLOT;
    }
  }
  STAMP(1);
  const u32 T = ~__builtin_amdgcn_readfirstlane(Ev);
  const i32x4 sws = make_srd(ws, WS_END);
  const f32x2 bv[4] = {{lo(bA.x), hi(bA.x)}, {lo(bA.y), hi(bA.y)}, {lo(bB.x), hi(bB.x)}, {lo(bB.y), hi(bB.y)}};
  u32 vo = (u32)lane * 16u;
  const u32 S = ((T >> 15) & 1u) * 0x80008000u;
  const u32 hb = WS_H + (u32)(mb * 4 + s) * (5u * 36864u) + (u32)w * 1024u;
  u32 so[9];
#pragma unroll
  for (int i = 0; i < 9; ++i) so[i] = hb + (u32)(g + i >= 9 ? g + i - 9 : g + i) * 4096u;
  bf16x8 W[9][4];
  u32x4 H[9];
  const u32 wb = (u32)(32 * w + 4 * q + a) * 128u + (u32)((2 * q + (a >> 1)) & 3) * 32u + (u32)p * 8u;
#pragma unroll
  for (int i = 0; i < 9; ++i) {
    if (i < 5) {
      u32x4 h;
      h.x = act2(acc[0][i].x, acc[0][i].y, bv[0]) | S; h.y = act2(acc[0][i].z, acc[0][i].w, bv[1]) | S;
      h.z = act2(acc[1][i].x, acc[1][i].y, bv[2]) | S; h.w = act2(acc[1][i].z, acc[1][i].w, bv[3]) | S;
      if (i < 4 || nm == 5) __builtin_amdgcn_raw_buffer_store_b128(h, rw, lane * 16, so[0] + i * 36864u, 16);
      const int k = i * 4 + w;
      *(AS3 u32x4*)(L + (3 * SLOT + 16384 + k * 1024 + (k >= 10 ? 16384 : 0)) + lane * 16) = h;
      if (i == 4) STAMP(2);
    }
    BAR_L();
#pragma unroll
    for (int t = 0; t < 4; ++t) {
      const lptr pw = L + (wb ^ (t * 32u)) + ((9 + i) % 6) * SLOT;
      W[i][t] = tr2(pw, pw + 2048);
    }
    if (i == HPRE) {
      asm volatile("s_nop 4");
      HLOAD(1, 224, 227); HLOAD(2, 228, 231); HLOAD(3, 232, 235); HLOAD(4, 236, 239);
      HLOAD(5, 240, 243); HLOAD(6, 244, 247); HLOAD(7, 248, 251); HLOAD(8, 252, 255);
    }
    __builtin_amdgcn_sched_barrier(0);
  }
  STAMP(3);
  u32 cnt = SPIN_MAX, t0, t1;
  asm volatile("s_waitcnt vmcnt(7)\n\t" CHK_A("v224", "v225", "v226", "v227") CHK_B RETRY("v[224:227]", "v224", "v225", "v226", "v227")
               : [t0] "=&v"(t0), [t1] "=&v"(t1), [cnt] "+s"(cnt), "+{v[224:227]}"(H[1])
               : [S] "s"(S), [M] "s"(0x80008000u), [vr] "v"(vo), [rw] "s"(sws), [sn] "s"(so[1]) : "memory", "vcc", "scc");
#pragma unroll 1
  for (int mt = 0; mt < nm; ++mt) {
    const int k = mt * 4 + w;
    H[0] = *(AS3 u32x4*)(L + (3 * SLOT + 16384 + k * 1024 + (k >= 10 ? 16384 : 0)) + lane * 16);
    const u32 vn = mt + 1 < nm ? vo + 36864u : 0x80000000u;
    const u32 mn = mt + 1 < nm ? 0x80008000u : 0u;
    f32x4 o[4];
    STEP_X(1, 224, 227, 228, 229, 230, 231, "0", "0", "0", "0", "=&v");
    STEP_X(2, 228, 231, 232, 233, 234, 235, ACC);
    STEP_X(3, 232, 235, 236, 237, 238, 239, ACC);
    STEP_X(4, 236, 239, 240, 241, 242, 243, ACC);
    STEP_X(5, 240, 243, 244, 245, 246, 247, ACC);
    STEP_X(6, 244, 247, 248, 249, 250, 251, ACC);
    STEP_X(7, 248, 251, 252, 253, 254, 255, ACC);
    asm volatile(MF("%[o0]", "%[w0]", "v[252:255]", "%[o0]") MF("%[o1]", "%[w1]", "v[252:255]", "%[o1]")
                 MF("%[o2]", "%[w2]", "v[252:255]", "%[o2]") MF("%[o3]", "%[w3]", "v[252:255]", "%[o3]")
                 "buffer_load_dwordx4 v[252:255], %[vn], %[rw], %[sc] offen sc1"
                 : [o0] "+v"(o[0]), [o1] "+v"(o[1]), [o2] "+v"(o[2]), [o3] "+v"(o[3]), "+{v[252:255]}"(H[8])
                 : [w0] "v"(W[8][0]), [w1] "v"(W[8][1]), [w2] "v"(W[8][2]), [w3] "v"(W[8][3]), [vn] "v"(vn), [rw] "s"(sws), [sc] "s"(so[8])
                 : "memory");
    asm volatile(MF("%[o0]", "%[w0]", "%[h]", "%[o0]") "s_waitcnt vmcnt(7)\n\t" MF("%[o1]", "%[w1]", "%[h]", "%[o1]")
                 CHK_A("v224", "v225", "v226", "v227") MF("%[o2]", "%[w2]", "%[h]", "%[o2]") CHK_B
                 RETRY("v[224:227]", "v224", "v225", "v226", "v227") MF("%[o3]", "%[w3]", "%[h]", "%[o3]") "s_nop 7"
                 : [o0] "+v"(o[0]), [o1] "+v"(o[1]), [o2] "+v"(o[2]), [o3] "+v"(o[3]), [t0] "=&v"(t0), [t1] "=&v"(t1),
                   [cnt] "+s"(cnt), "+{v[224:227]}"(H[1])
                 : [w0] "v"(W[0][0]), [w1] "v"(W[0][1]), [w2] "v"(W[0][2]), [w3] "v"(W[0][3]), [h] "v"(H[0]), [S] "s"(S),
                   [M] "s"(mn), [vr] "v"(vn), [rw] "s"(sws), [sn] "s"(so[1])
                 : "memory", "vcc", "scc");
    const lptr r = L + mt * SLOT + w * 4096 + lane * 16;
#pragma unroll
    for (int t = 0; t < 4; ++t) *(AS3 f32x4*)(r + t * 1024) = o[t];
    asm volatile("" ::: "memory");
    if (lane == 0) *(volatile AS3 u32*)(L + LDS_FLAGS + mt * 16 + w * 4) = 1u;
    vo = vn;
    if (mt == 0) STAMP(4);
  }
  STAMP(8);
  FLUSH();
}

__global__ void __launch_bounds__(64) report_kernel(char* ws) {
  const u64* t = (const u64*)(ws + WS_END);
  const int lane = threadIdx.x;
  u64 lo = ~0ull, lomax = 0, hi = 0;
  for (int b = lane; b < 256; b += 64) {
    int idx = (b & 1) * 32 + (b >> 3); if (idx >= 63) continue;
    for (int w = 0; w < 8; ++w) { u64 a = t[(b * 8 + w) * 32 + 12], e = t[(b * 8 + w) * 32 + 13]; lo = a < lo ? a : lo; lomax = a > lomax ? a : lomax; hi = e > hi ? e : hi; }
  }
  for (int o = 32; o; o >>= 1) { u64 a = __shfl_xor(lo, o), c = __shfl_xor(lomax, o), d = __shfl_xor(hi, o); lo = a < lo ? a : lo; lomax = c > lomax ? c : lomax; hi = d > hi ? d : hi; }
  const int W[10] = {4, 4, 4, 0, 0, 0, 0, 0, 6, -1};
  const int K[10] = {1, 2, 3, 1, 2, 3, 4, 8, 8, 10};
  int mean[10], mx[10];
#pragma unroll
  for (int i = 0; i < 10; ++i) {
    int sum = 0, cnt = 0, m = 0;
    for (int b = lane; b < 256; b += 64) {
      int idx = (b & 1) * 32 + (b >> 3); if (idx >= 63) continue;
      int w = W[i] < 0 ? 4 + ((b & 7) >> 1) : W[i];
      int d = (int)(t[(b * 8 + w) * 32 + K[i]] - lo);
      sum += d; ++cnt; m = d > m ? d : m;
    }
    for (int o = 32; o; o >>= 1) { sum += __shfl_xor(sum, o); cnt += __shfl_xor(cnt, o); int c = __shfl_xor(m, o); m = c > m ? c : m; }
    mean[i] = sum / cnt; mx[i] = m;
  }
  if (lane == 0)
    printf("R sp %d span %d | iss %d/%d b7 %d/%d lend %d/%d p1 %d/%d epi %d/%d w2 %d/%d mt0 %d/%d cend %d/%d hl %d/%d fin %d/%d\n", (int)(lomax - lo), (int)(hi - lo),
           mean[0], mx[0], mean[1], mx[1], mean[2], mx[2], mean[3], mx[3], mean[4], mx[4], mean[5], mx[5], mean[6], mx[6], mean[7], mx[7], mean[8], mx[8], mean[9], mx[9]);
}

extern "C" void kernel_launch(void* const* d_in, const int* in_sizes, int n_in, void* d_out, int out_size, void* d_ws,
                              size_t ws_size, hipStream_t stream) {
  (void)in_sizes; (void)n_in; (void)out_size; (void)ws_size;
  (void)hipFuncSetAttribute((const void*)ffn_kernel, hipFuncAttributeMaxDynamicSharedMemorySize, LDS_BYTES);
  ffn_kernel<<<256, 512, LDS_BYTES, stream>>>((const u16*)d_in[0], (const u16*)d_in[1], (const u16*)d_in[2],
                                              (const u16*)d_in[3], (const u16*)d_in[4], (u16*)d_out, (char*)d_ws);
  report_kernel<<<1, 64, 0, stream>>>((char*)d_ws);
}
```

```cpp
#include <hip/hip_runtime.h>
#define PROF 1
#define PW -1
#define PK 12

typedef unsigned short u16;
typedef unsigned int u32;
typedef unsigned long long u64;
typedef __bf16 bf16x8 __attribute__((ext_vector_type(8)));
typedef __bf16 bf16x2 __attribute__((ext_vector_type(2)));
typedef short s16x4 __attribute__((ext_vector_type(4)));
typedef short s16x8 __attribute__((ext_vector_type(8)));
typedef float f32x4 __attribute__((ext_vector_type(4)));
typedef float f32x2 __attribute__((ext_vector_type(2)));
typedef u32 u32x4 __attribute__((ext_vector_type(4)));
typedef u32 u32x2 __attribute__((ext_vector_type(2)));
typedef int i32x4 __attribute__((ext_vector_type(4)));
#define AS3 __attribute__((address_space(3)))
#define AS4 __attribute__((address_space(4)))
typedef AS3 char* lptr;

constexpr int SLOT = 26 * 1024;
constexpr int LDS_FLAGS = 6 * SLOT;
constexpr int LDS_BYTES = LDS_FLAGS + 256;
constexpr u32 WS_H = 4096;
constexpr u32 WS_P = WS_H + 7u * 4 * 5 * 36 * 1024;
constexpr u32 WS_END = WS_P + 7u * 9 * 16 * 5 * 1024;
constexpr int SPIN_MAX = 1 << 16;
#define ROT 1
#define HPRE 6

#define STAMP(k) asm volatile("s_memrealtime %0\n\ts_waitcnt lgkmcnt(0)" : "=s"(ts[k]) :: "memory")
#define STAMPR(k) asm volatile("s_memrealtime %0\n\ts_waitcnt lgkmcnt(0)" : "=s"(ts[k]) :: "memory")
#define FLUSH() do { if (PW < 0 || wave == PW || (PW == 99 && wave - 4 == s)) { u64 t_; do { asm volatile("s_sleep 1\n\ts_memrealtime %0\n\ts_waitcnt lgkmcnt(0)" : "=s"(t_) :: "memory"); } while (t_ < ts[PK] + 3000u); } } while (0)

__device__ __forceinline__ i32x4 make_srd(const void* p, u32 bytes) {
  u64 a = (u64)p;
  i32x4 r; r.x = (int)(u32)a; r.y = (int)(u32)(a >> 32) & 0xffff; r.z = (int)bytes; r.w = 0x00020000;
  return r;
}
#define BAR() asm volatile("s_barrier" ::: "memory")
#define BAR_L() asm volatile("s_waitcnt lgkmcnt(0)\n\ts_barrier" ::: "memory")
#define WAITV_BAR(n) asm volatile("s_waitcnt vmcnt(" #n ")\n\ts_barrier" ::: "memory")

#define DMA(v, s, o) "buffer_load_dwordx4 " v ", " s ", " o " offen lds\n\t"
#define STEP(o, ms, os) "s_add_u32 m0, m0, " #ms "\n\ts_add_u32 " o ", " o ", " #os "\n\t"
__device__ __forceinline__ void issue1(i32x4 sx, i32x4 sw, u32 vx, u32 vw, u32 ox, u32 ow, u32 mx, u32 mw) {
  asm volatile(
      "s_nop 4\n\t"
      "s_mov_b32 m0, %7\n\ts_nop 0\n\t"
      DMA("%3", "%5", "%1") STEP("%1", 1024, 36864) DMA("%3", "%5", "%1") STEP("%1", 3072, 110592)
      DMA("%3", "%5", "%1") STEP("%1", 1024, 36864) DMA("%3", "%5", "%1") STEP("%1", 3072, 110592)
      DMA("%3", "%5", "%1") STEP("%1", 1024, 36864) DMA("%3", "%5", "%1") STEP("%1", 3072, 110592)
      DMA("%3", "%5", "%1") STEP("%1", 1024, 36864) DMA("%3", "%5", "%1")
      "s_mov_b32 m0, %6\n\ts_nop 0\n\t"
      DMA("%2", "%4", "%0") STEP("%0", 2048, 18432) DMA("%2", "%4", "%0") STEP("%0", 2048, 18432)
      DMA("%2", "%4", "%0") STEP("%0", 2048, 18432) DMA("%2", "%4", "%0") STEP("%0", 2048, 18432)
      DMA("%2", "%4", "%0")
      : "+s"(ox), "+s"(ow) : "v"(vx), "v"(vw), "s"(sx), "s"(sw), "s"(mx), "s"(mw) : "memory", "scc");
}
__device__ __forceinline__ void issue2(i32x4 sw, u32 v, u32 o, u32 m) {
  asm volatile(
      "s_nop 4\n\t"
      "s_mov_b32 m0, %3\n\ts_nop 0\n\t"
      DMA("%1", "%2", "%0") STEP("%0", 2048, 18432) DMA("%1", "%2", "%0") STEP("%0", 2048, 18432)
      DMA("%1", "%2", "%0") STEP("%0", 2048, 18432) DMA("%1", "%2", "%0") STEP("%0", 2048, 18432)
      DMA("%1", "%2", "%0") STEP("%0", 2048, 18432) DMA("%1", "%2", "%0") STEP("%0", 2048, 18432)
      DMA("%1", "%2", "%0") STEP("%0", 2048, 18432) DMA("%1", "%2", "%0")
      : "+s"(o) : "v"(v), "s"(sw), "s"(m) : "memory", "scc");
}
__device__ __forceinline__ bf16x8 tr2(lptr p0, lptr p1) {
  s16x4 a = __builtin_amdgcn_ds_read_tr16_b64_v4i16((AS3 s16x4*)p0);
  s16x4 b = __builtin_amdgcn_ds_read_tr16_b64_v4i16((AS3 s16x4*)p1);
  s16x8 c = __builtin_shufflevector(a, b, 0, 1, 2, 3, 4, 5, 6, 7);
  return __builtin_bit_cast(bf16x8, c);
}
__device__ __forceinline__ u32 pk(float a, float b) {
  bf16x2 v = {(__bf16)a, (__bf16)b};
  return __builtin_bit_cast(u32, v);
}
__device__ __forceinline__ float lo(u32 p) { return __uint_as_float(p << 16); }
__device__ __forceinline__ float hi(u32 p) { return __uint_as_float(p & 0xffff0000u); }
__device__ __forceinline__ float relu(float v) {
  float r; asm("v_max_f32 %0, 0, %1" : "=v"(r) : "v"(v)); return r;
}
__device__ __forceinline__ u32 act2(float a0, float a1, f32x2 bv) {
  u32 p = pk(a0, a1);
  asm("" : "+v"(p));
  f32x2 v = {lo(p), hi(p)};
  v += bv;
  p = pk(v.x, v.y);
  asm("" : "+v"(p));
  f32x2 r = {relu(lo(p)), relu(hi(p))};
  r *= r;
  return pk(r.x, r.y);
}
#define MF(o, wv, h, c) "v_mfma_f32_16x16x32_bf16 " o ", " wv ", " h ", " c "\n\t"
#define CHK_A(n0, n1, n2, n3) "v_bitop3_b32 %[t0], " n0 ", " n1 ", %[S] bitop3:0x7e\n\tv_bitop3_b32 %[t1], " n2 ", " n3 ", %[S] bitop3:0x7e\n\t"
#define CHK_B "v_bitop3_b32 %[t0], %[t0], %[t1], %[M] bitop3:0xa8\n\tv_cmp_ne_u32 vcc, 0, %[t0]\n\t"
#define RETRY(n, n0, n1, n2, n3) \
  "s_cbranch_vccz 2f\n\t" \
  "1:\n\ts_cmp_eq_u32 %[cnt], 0\n\ts_cbranch_scc1 2f\n\ts_sub_u32 %[cnt], %[cnt], 1\n\t" \
  "buffer_load_dwordx4 " n ", %[vr], %[rw], %[sn] offen sc1\n\ts_waitcnt vmcnt(0)\n\t" \
  CHK_A(n0, n1, n2, n3) CHK_B "s_cbranch_vccnz 1b\n\t2:\n\t"
#define HR(a, b) "v[" #a ":" #b "]"
#define VR(a) "v" #a
#define STEP_MID(I, c0, c3, n0, n1, n2, n3, C0, C1, C2, C3, OC) \
  asm volatile(MF("%[o0]", "%[w0]", HR(c0, c3), C0) "s_waitcnt vmcnt(6)\n\t" MF("%[o1]", "%[w1]", HR(c0, c3), C1) \
      CHK_A(VR(n0), VR(n1), VR(n2), VR(n3)) MF("%[o2]", "%[w2]", HR(c0, c3), C2) CHK_B \
      RETRY(HR(n0, n3), VR(n0), VR(n1), VR(n2), VR(n3)) MF("%[o3]", "%[w3]", HR(c0, c3), C3) \
      "buffer_load_dwordx4 " HR(c0, c3) ", %[vn], %[rw], %[sc] offen sc1" \
      : [o0] OC(o[0]), [o1] OC(o[1]), [o2] OC(o[2]), [o3] OC(o[3]), [t0] "=&v"(t0), [t1] "=&v"(t1), [cnt] "+s"(cnt), \
        "+{" HR(c0, c3) "}"(H[I]), "+{" HR(n0, n3) "}"(H[I + 1]) \
      : [w0] "v"(W[I][0]), [w1] "v"(W[I][1]), [w2] "v"(W[I][2]), [w3] "v"(W[I][3]), [S] "s"(S), [M] "s"(0x80008000u), \
        [vr] "v"(vo), [vn] "v"(vn), [rw] "s"(sws), [sn] "s"(so[I + 1]), [sc] "s"(so[I]) \
      : "memory", "vcc", "scc")
#define ACC "%[o0]", "%[o1]", "%[o2]", "%[o3]", "+v"
#define STEP_X(...) STEP_MID(__VA_ARGS__)
#define HLOAD(I, c0, c3) \
  asm volatile("buffer_load_dwordx4 " HR(c0, c3) ", %1, %2, %3 offen sc1" : "={" HR(c0, c3) "}"(H[I]) : "v"(vo), "s"(sws), "s"(so[I]) : "memory")
__device__ __forceinline__ bool any_lane(bool c) { return __builtin_amdgcn_ballot_w64(c) != 0; }

__global__ void __launch_bounds__(512, 2)
ffn_kernel(const u16* __restrict__ x, const u16* __restrict__ w1, const u16* __restrict__ b1,
           const u16* __restrict__ w2, const u16* __restrict__ b2, u16* __restrict__ out, char* ws) {
  extern __shared__ __attribute__((aligned(16))) char lds_[];
  const lptr L = (lptr)lds_;
  u64 ts[14] = {0, 0, 0, 0, 0, 0, 0, 0, 0, 0, 0, 0, 0, 0};
  STAMPR(12);
  const int b = blockIdx.x;
  const int s = (b & 7) >> 1, idx = (b & 1) * 32 + (b >> 3);
  if (idx >= 63) return;
  const int mb = idx / 9, g = idx - mb * 9;
  const int mt0 = mb < 4 ? 5 * mb : 4 * mb + 4, nm = mb < 4 ? 5 : 4;
  const int wave = __builtin_amdgcn_readfirstlane(threadIdx.x >> 6), lane = threadIdx.x & 63;
  STAMP(0);
  asm volatile("" :: "s"(x), "s"(w1), "s"(w2), "s"(ws), "s"(b1));
  const __amdgpu_buffer_rsrc_t rw = __builtin_amdgcn_make_buffer_rsrc(ws, 0, WS_END, 0x00020000);
  const int q = lane >> 4;

  if (wave >= 4) {
    const int u = wave - 4;
    if (u < 2) {
      __builtin_amdgcn_s_setprio(3);
      const i32x4 sx = make_srd(x, (u32)(mt0 + nm) * 16u * 1152u), sw1 = make_srd(w1, 576u * 9216u), sw2 = make_srd(w2, 4608u * 1152u);
      const u32 par = (u32)u;
      const u32 vx = (u32)(lane >> 3) * 1152u + (u32)((lane & 7) ^ (4 * par + (lane >> 4))) * 16u;
      const u32 vw = (u32)(lane >> 4) * 9216u + (u32)(((lane & 15) >> 1) ^ ((lane >> 4) | (par << 2))) * 32u + (u32)(lane & 1) * 16u;
      const u32 v2 = (u32)(lane >> 3) * 1152u + (u32)(((lane & 7) >> 1) ^ ((lane >> 4) & 3)) * 32u + (u32)(lane & 1) * 16u;
      const u32 oxb = (u32)(mt0 * 16 + 8 * par) * 1152u, owb = par * 8u * 9216u + (u32)(s * 9 + g) * 256u;
      const u32 o2b = (u32)(s * 1152 + 8 * par) * 1152u + (u32)g * 128u;
      const u32 mxb = par * 1024u, mwb = 10240u + par * 2048u;
      u32 kp = ROT ? (u32)(mb * 4 / 3) : 0u, slot = 0;
      auto st1 = [&]() {
        issue1(sx, sw1, vx, vw, oxb + kp * 128u, owb + kp * (64u * 9216u), mxb + slot, mwb + slot);
        kp = kp == 8u ? 0u : kp + 1u; slot = slot == 5u * SLOT ? 0u : slot + SLOT;
      };
      u32 gp = (u32)g;
      auto st2 = [&]() {
        issue2(sw2, v2, o2b + gp * (128u * 1152u), mxb + slot);
        gp = gp == 8u ? 0u : gp + 1u; slot = slot == 5u * SLOT ? 0u : slot + SLOT;
      };
      st1(); st1();
      STAMP(1);
#pragma unroll 1
      for (int j = 0; j < 7; ++j) { WAITV_BAR(13); st1(); }
      WAITV_BAR(13); st2(); st2();
      STAMP(2);
#pragma unroll 1
      for (int j = 8; j < 15; ++j) { WAITV_BAR(16); st2(); }
      WAITV_BAR(16); WAITV_BAR(8); WAITV_BAR(0);
      STAMP(3);
      __builtin_amdgcn_s_setprio(0);
    } else {
      const u32 Ev = __builtin_amdgcn_raw_buffer_load_b32(rw, 0, mb * 256, 16);
      if (lane < 20) *(AS3 u32*)(L + LDS_FLAGS + lane * 4) = 0u;
      BAR_L();
#pragma unroll 1
      for (int j = 1; j < 7; ++j) BAR();
      *(AS3 u32*)(L + LDS_FLAGS + 128) = ~Ev;
      BAR_L();
#pragma unroll 1
      for (int j = 8; j < 18; ++j) BAR();
    }
    const u32 T = __builtin_amdgcn_readfirstlane(*(AS3 u32*)(L + LDS_FLAGS + 128));
    const u32 sgn = (T << 16) & 0x80000000u, nib = T & 15u;
    u32x2 bb = {0u, 0u};
    if (u == s) bb = *(const u32x2*)(b2 + g * 64 + s * 16 + q * 4);
    const u32 pout = WS_P + (u32)(((mb * 9 + g) * 4 + u) * 4 + s) * 5120u;
#pragma unroll 1
    for (int mt = 0; mt < nm; ++mt) {
#pragma unroll 1
      for (int it = 0; it < (SPIN_MAX << 6); ++it) {
        const u32x4 f = *(volatile AS3 u32x4*)(L + LDS_FLAGS + mt * 16);
        if (__builtin_amdgcn_readfirstlane(f.x & f.y & f.z & f.w) == 1u) break;
        __builtin_amdgcn_s_sleep(1);
      }
      asm volatile("" ::: "memory");
      const lptr r = L + mt * SLOT + u * 1024 + lane * 16;
      f32x4 v = *(AS3 f32x4*)r;
      v += *(AS3 f32x4*)(r + 4096); v += *(AS3 f32x4*)(r + 8192); v += *(AS3 f32x4*)(r + 12288);
      u32x4 w = __builtin_bit_cast(u32x4, v) ^ sgn;
      if (u == s) *(AS3 u32x4*)r = w;
      else __builtin_amdgcn_raw_buffer_store_b128((w & ~15u) | nib, rw, lane * 16, pout + mt * 1024, 16);
      if (mt == 0) STAMP(4);
    }
    STAMP(8);
    if (u == s) {
      const u32 pin = WS_P + (u32)((mb * 9 + g) * 4 + s) * 4 * 5120u;
      u32 so[3];
#pragma unroll
      for (int k = 0; k < 3; ++k) so[k] = pin + (u32)(k + (k >= s ? 1 : 0)) * 5120u;
      u32x4 p[5][3];
#pragma unroll
      for (int mt = 0; mt < 5; ++mt) {
        const u32 vo = (mt < 4 || nm == 5) ? (u32)lane * 16u + mt * 1024u : 0x80000000u;
#pragma unroll
        for (int k = 0; k < 3; ++k) p[mt][k] = __builtin_amdgcn_raw_buffer_load_b128(rw, vo, so[k], 16);
      }
#pragma unroll
      for (int mt = 0; mt < 5; ++mt) {
        if (mt < nm) {
#pragma unroll 1
          for (int it = 0; it < SPIN_MAX; ++it) {
            u32 bad = 0;
#pragma unroll
            for (int k = 0; k < 3; ++k) bad |= (p[mt][k].x ^ nib) | (p[mt][k].y ^ nib) | (p[mt][k].z ^ nib) | (p[mt][k].w ^ nib);
            if (!any_lane((bad & 15u) != 0u)) break;
            asm volatile("" ::: "memory");
#pragma unroll
            for (int k = 0; k < 3; ++k) p[mt][k] = __builtin_amdgcn_raw_buffer_load_b128(rw, lane * 16 + mt * 1024, so[k], 16);
          }
          f32x4 v = *(AS3 f32x4*)(L + mt * SLOT + u * 1024 + lane * 16);
#pragma unroll
          for (int k = 0; k < 3; ++k) v += __builtin_bit_cast(f32x4, p[mt][k] & ~15u);
          const u32 p0 = pk(v.x, v.y), p1 = pk(v.z, v.w);
          u32x2 o;
          o.x = pk(lo(p0) + lo(bb.x), hi(p0) + hi(bb.x));
          o.y = pk(lo(p1) + lo(bb.y), hi(p1) + hi(bb.y));
          *(u32x2*)(out + (size_t)((mt0 + mt) * 16 + (lane & 15)) * 576 + g * 64 + s * 16 + q * 4) = o;
        }
      }
      if (s == 0 && g == 0 && lane == 0) __builtin_amdgcn_raw_buffer_store_b32(T, rw, 0, mb * 256, 16);
      STAMP(10);
    }
    FLUSH();
    return;
  }

  const int w = wave;
  const int a = (lane >> 2) & 3, p = lane & 3, m = lane & 15;
  const u32x2 bA = *(const u32x2*)(b1 + (s * 9 + g) * 128 + w * 32 + q * 4);
  const u32x2 bB = *(const u32x2*)(b1 + (s * 9 + g) * 128 + w * 32 + 16 + q * 4);
  const u32 Ev = __builtin_amdgcn_raw_buffer_load_b32(rw, 0, mb * 256, 16);
  f32x4 acc[2][5];
#pragma unroll
  for (int t = 0; t < 2; ++t)
#pragma unroll
    for (int mt = 0; mt < 5; ++mt) acc[t][mt] = (f32x4){0.f, 0.f, 0.f, 0.f};
  {
    const u32 xa0 = (u32)m * 128u + (u32)(q ^ (m >> 1)) * 16u;
    const u32 wa0 = 10240u + (u32)(8 * q + a) * 256u + (u32)((2 * w) ^ (a | ((q & 1) << 2))) * 32u + (u32)p * 8u;
    u32 slot = 0;
#pragma unroll 1
    for (int j = 0; j < 9; ++j) {
      BAR();
      bf16x8 A[2][2], B[2][5];
#pragma unroll
      for (int ks = 0; ks < 2; ++ks) {
#pragma unroll
        for (int t = 0; t < 2; ++t) {
          const lptr pw = L + (slot + (wa0 ^ (t * 32u))) + ks * 8192;
          A[ks][t] = tr2(pw, pw + 1024);
        }
#pragma unroll
        for (int mt = 0; mt < 5; ++mt) B[ks][mt] = *(AS3 bf16x8*)(L + (slot + (xa0 ^ (ks * 64u))) + mt * 2048);
        __builtin_amdgcn_sched_barrier(0);
      }
#pragma unroll
      for (int ks = 0; ks < 2; ++ks)
#pragma unroll
        for (int mt = 0; mt < 5; ++mt)
#pragma unroll
          for (int t = 0; t < 2; ++t) acc[t][mt] = __builtin_amdgcn_mfma_f32_16x16x32_bf16(A[ks][t], B[ks][mt], acc[t][mt], 0, 0, 0);
      slot = slot == 5u * SLOT ? 0u : slot + SLOT;
    }
  }
  STAMP(1);
  const u32 T = ~__builtin_amdgcn_readfirstlane(Ev);
  const i32x4 sws = make_srd(ws, WS_END);
  const f32x2 bv[4] = {{lo(bA.x), hi(bA.x)}, {lo(bA.y), hi(bA.y)}, {lo(bB.x), hi(bB.x)}, {lo(bB.y), hi(bB.y)}};
  u32 vo = (u32)lane * 16u;
  const u32 S = ((T >> 15) & 1u) * 0x80008000u;
  const u32 hb = WS_H + (u32)(mb * 4 + s) * (5u * 36864u) + (u32)w * 1024u;
  u32 so[9];
#pragma unroll
  for (int i = 0; i < 9; ++i) so[i] = hb + (u32)(g + i >= 9 ? g + i - 9 : g + i) * 4096u;
  bf16x8 W[9][4];
  u32x4 H[9];
  const u32 wb = (u32)(32 * w + 4 * q + a) * 128u + (u32)((2 * q + (a >> 1)) & 3) * 32u + (u32)p * 8u;
#pragma unroll
  for (int i = 0; i < 9; ++i) {
    if (i < 5) {
      u32x4 h;
      h.x = act2(acc[0][i].x, acc[0][i].y, bv[0]) | S; h.y = act2(acc[0][i].z, acc[0][i].w, bv[1]) | S;
      h.z = act2(acc[1][i].x, acc[1][i].y, bv[2]) | S; h.w = act2(acc[1][i].z, acc[1][i].w, bv[3]) | S;
      if (i < 4 || nm == 5) __builtin_amdgcn_raw_buffer_store_b128(h, rw, lane * 16, so[0] + i * 36864u, 16);
      const int k = i * 4 + w;
      *(AS3 u32x4*)(L + (3 * SLOT + 16384 + k * 1024 + (k >= 10 ? 16384 : 0)) + lane * 16) = h;
      if (i == 4) STAMP(2);
    }
    BAR_L();
#pragma unroll
    for (int t = 0; t < 4; ++t) {
      const lptr pw = L + (wb ^ (t * 32u)) + ((9 + i) % 6) * SLOT;
      W[i][t] = tr2(pw, pw + 2048);
    }
    if (i == HPRE) {
      asm volatile("s_nop 4");
      HLOAD(1, 224, 227); HLOAD(2, 228, 231); HLOAD(3, 232, 235); HLOAD(4, 236, 239);
      HLOAD(5, 240, 243); HLOAD(6, 244, 247); HLOAD(7, 248, 251); HLOAD(8, 252, 255);
    }
    __builtin_amdgcn_sched_barrier(0);
  }
  STAMP(3);
  u32 cnt = SPIN_MAX, t0, t1;
  asm volatile("s_waitcnt vmcnt(7)\n\t" CHK_A("v224", "v225", "v226", "v227") CHK_B RETRY("v[224:227]", "v224", "v225", "v226", "v227")
               : [t0] "=&v"(t0), [t1] "=&v"(t1), [cnt] "+s"(cnt), "+{v[224:227]}"(H[1])
               : [S] "s"(S), [M] "s"(0x80008000u), [vr] "v"(vo), [rw] "s"(sws), [sn] "s"(so[1]) : "memory", "vcc", "scc");
#pragma unroll 1
  for (int mt = 0; mt < nm; ++mt) {
    const int k = mt * 4 + w;
    H[0] = *(AS3 u32x4*)(L + (3 * SLOT + 16384 + k * 1024 + (k >= 10 ? 16384 : 0)) + lane * 16);
    const u32 vn = mt + 1 < nm ? vo + 36864u : 0x80000000u;
    const u32 mn = mt + 1 < nm ? 0x80008000u : 0u;
    f32x4 o[4];
    STEP_X(1, 224, 227, 228, 229, 230, 231, "0", "0", "0", "0", "=&v");
    STEP_X(2, 228, 231, 232, 233, 234, 235, ACC);
    STEP_X(3, 232, 235, 236, 237, 238, 239, ACC);
    STEP_X(4, 236, 239, 240, 241, 242, 243, ACC);
    STEP_X(5, 240, 243, 244, 245, 246, 247, ACC);
    STEP_X(6, 244, 247, 248, 249, 250, 251, ACC);
    STEP_X(7, 248, 251, 252, 253, 254, 255, ACC);
    asm volatile(MF("%[o0]", "%[w0]", "v[252:255]", "%[o0]") MF("%[o1]", "%[w1]", "v[252:255]", "%[o1]")
                 MF("%[o2]", "%[w2]", "v[252:255]", "%[o2]") MF("%[o3]", "%[w3]", "v[252:255]", "%[o3]")
                 "buffer_load_dwordx4 v[252:255], %[vn], %[rw], %[sc] offen sc1"
                 : [o0] "+v"(o[0]), [o1] "+v"(o[1]), [o2] "+v"(o[2]), [o3] "+v"(o[3]), "+{v[252:255]}"(H[8])
                 : [w0] "v"(W[8][0]), [w1] "v"(W[8][1]), [w2] "v"(W[8][2]), [w3] "v"(W[8][3]), [vn] "v"(vn), [rw] "s"(sws), [sc] "s"(so[8])
                 : "memory");
    asm volatile(MF("%[o0]", "%[w0]", "%[h]", "%[o0]") "s_waitcnt vmcnt(7)\n\t" MF("%[o1]", "%[w1]", "%[h]", "%[o1]")
                 CHK_A("v224", "v225", "v226", "v227") MF("%[o2]", "%[w2]", "%[h]", "%[o2]") CHK_B
                 RETRY("v[224:227]", "v224", "v225", "v226", "v227") MF("%[o3]", "%[w3]", "%[h]", "%[o3]") "s_nop 7"
                 : [o0] "+v"(o[0]), [o1] "+v"(o[1]), [o2] "+v"(o[2]), [o3] "+v"(o[3]), [t0] "=&v"(t0), [t1] "=&v"(t1),
                   [cnt] "+s"(cnt), "+{v[224:227]}"(H[1])
                 : [w0] "v"(W[0][0]), [w1] "v"(W[0][1]), [w2] "v"(W[0][2]), [w3] "v"(W[0][3]), [h] "v"(H[0]), [S] "s"(S),
                   [M] "s"(mn), [vr] "v"(vn), [rw] "s"(sws), [sn] "s"(so[1])
                 : "memory", "vcc", "scc");
    const lptr r = L + mt * SLOT + w * 4096 + lane * 16;
#pragma unroll
    for (int t = 0; t < 4; ++t) *(AS3 f32x4*)(r + t * 1024) = o[t];
    asm volatile("" ::: "memory");
    if (lane == 0) *(volatile AS3 u32*)(L + LDS_FLAGS + mt * 16 + w * 4) = 1u;
    vo = vn;
    if (mt == 0) STAMP(4);
  }
  STAMP(8);
  FLUSH();
}

extern "C" void kernel_launch(void* const* d_in, const int* in_sizes, int n_in, void* d_out, int out_size, void* d_ws,
                              size_t ws_size, hipStream_t stream) {
  (void)in_sizes; (void)n_in; (void)out_size; (void)ws_size;
  (void)hipFuncSetAttribute((const void*)ffn_kernel, hipFuncAttributeMaxDynamicSharedMemorySize, LDS_BYTES);
  ffn_kernel<<<256, 512, LDS_BYTES, stream>>>((const u16*)d_in[0], (const u16*)d_in[1], (const u16*)d_in[2],
                                              (const u16*)d_in[3], (const u16*)d_in[4], (u16*)d_out, (char*)d_ws);
}
```

```cpp
#include <hip/hip_runtime.h>

typedef unsigned short u16;
typedef unsigned int u32;
typedef unsigned long long u64;
typedef __bf16 bf16x8 __attribute__((ext_vector_type(8)));
typedef __bf16 bf16x2 __attribute__((ext_vector_type(2)));
typedef short s16x4 __attribute__((ext_vector_type(4)));
typedef short s16x8 __attribute__((ext_vector_type(8)));
typedef float f32x4 __attribute__((ext_vector_type(4)));
typedef float f32x2 __attribute__((ext_vector_type(2)));
typedef u32 u32x4 __attribute__((ext_vector_type(4)));
typedef u32 u32x2 __attribute__((ext_vector_type(2)));
typedef int i32x4 __attribute__((ext_vector_type(4)));
#define AS3 __attribute__((address_space(3)))
#define AS4 __attribute__((address_space(4)))
typedef AS3 char* lptr;

constexpr int SLOT = 26 * 1024;
constexpr int LDS_FLAGS = 6 * SLOT;
constexpr int LDS_BYTES = LDS_FLAGS + 256;
constexpr u32 WS_H = 4096;
constexpr u32 WS_P = WS_H + 7u * 4 * 5 * 36 * 1024;
constexpr u32 WS_END = WS_P + 7u * 9 * 16 * 5 * 1024;
constexpr int SPIN_MAX = 1 << 16;
#define ROT 1
#define HPRE 6

#define STAMP(k) do { } while (0)

__device__ __forceinline__ i32x4 make_srd(const void* p, u32 bytes) {
  u64 a = (u64)p;
  i32x4 r; r.x = (int)(u32)a; r.y = (int)(u32)(a >> 32) & 0xffff; r.z = (int)bytes; r.w = 0x00020000;
  return r;
}
#define BAR() asm volatile("s_barrier" ::: "memory")
#define BAR_L() asm volatile("s_waitcnt lgkmcnt(0)\n\ts_barrier" ::: "memory")
#define WAITV_BAR(n) asm volatile("s_waitcnt vmcnt(" #n ")\n\ts_barrier" ::: "memory")

#define DMA(v, s, o) "buffer_load_dwordx4 " v ", " s ", " o " offen lds\n\t"
#define STEP(o, ms, os) "s_add_u32 m0, m0, " #ms "\n\ts_add_u32 " o ", " o ", " #os "\n\t"
__device__ __forceinline__ void issue1(i32x4 sx, i32x4 sw, u32 vx, u32 vw, u32 ox, u32 ow, u32 mx, u32 mw) {
  asm volatile(
      "s_nop 4\n\t"
      "s_mov_b32 m0, %7\n\ts_nop 0\n\t"
      DMA("%3", "%5", "%1") STEP("%1", 1024, 36864) DMA("%3", "%5", "%1") STEP("%1", 3072, 110592)
      DMA("%3", "%5", "%1") STEP("%1", 1024, 36864) DMA("%3", "%5", "%1") STEP("%1", 3072, 110592)
      DMA("%3", "%5", "%1") STEP("%1", 1024, 36864) DMA("%3", "%5", "%1") STEP("%1", 3072, 110592)
      DMA("%3", "%5", "%1") STEP("%1", 1024, 36864) DMA("%3", "%5", "%1")
      "s_mov_b32 m0, %6\n\ts_nop 0\n\t"
      DMA("%2", "%4", "%0") STEP("%0", 2048, 18432) DMA("%2", "%4", "%0") STEP("%0", 2048, 18432)
      DMA("%2", "%4", "%0") STEP("%0", 2048, 18432) DMA("%2", "%4", "%0") STEP("%0", 2048, 18432)
      DMA("%2", "%4", "%0")
      : "+s"(ox), "+s"(ow) : "v"(vx), "v"(vw), "s"(sx), "s"(sw), "s"(mx), "s"(mw) : "memory", "scc");
}
__device__ __forceinline__ void issue2(i32x4 sw, u32 v, u32 o, u32 m) {
  asm volatile(
      "s_nop 4\n\t"
      "s_mov_b32 m0, %3\n\ts_nop 0\n\t"
      DMA("%1", "%2", "%0") STEP("%0", 2048, 18432) DMA("%1", "%2", "%0") STEP("%0", 2048, 18432)
      DMA("%1", "%2", "%0") STEP("%0", 2048, 18432) DMA("%1", "%2", "%0") STEP("%0", 2048, 18432)
      DMA("%1", "%2", "%0") STEP("%0", 2048, 18432) DMA("%1", "%2", "%0") STEP("%0", 2048, 18432)
      DMA("%1", "%2", "%0") STEP("%0", 2048, 18432) DMA("%1", "%2", "%0")
      : "+s"(o) : "v"(v), "s"(sw), "s"(m) : "memory", "scc");
}
__device__ __forceinline__ bf16x8 tr2(lptr p0, lptr p1) {
  s16x4 a = __builtin_amdgcn_ds_read_tr16_b64_v4i16((AS3 s16x4*)p0);
  s16x4 b = __builtin_amdgcn_ds_read_tr16_b64_v4i16((AS3 s16x4*)p1);
  s16x8 c = __builtin_shufflevector(a, b, 0, 1, 2, 3, 4, 5, 6, 7);
  return __builtin_bit_cast(bf16x8, c);
}
__device__ __forceinline__ u32 pk(float a, float b) {
  bf16x2 v = {(__bf16)a, (__bf16)b};
  return __builtin_bit_cast(u32, v);
}
__device__ __forceinline__ float lo(u32 p) { return __uint_as_float(p << 16); }
__device__ __forceinline__ float hi(u32 p) { return __uint_as_float(p & 0xffff0000u); }
__device__ __forceinline__ float relu(float v) {
  float r; asm("v_max_f32 %0, 0, %1" : "=v"(r) : "v"(v)); return r;
}
__device__ __forceinline__ u32 act2(float a0, float a1, f32x2 bv) {
  u32 p = pk(a0, a1);
  asm("" : "+v"(p));
  f32x2 v = {lo(p), hi(p)};
  v += bv;
  p = pk(v.x, v.y);
  asm("" : "+v"(p));
  f32x2 r = {relu(lo(p)), relu(hi(p))};
  r *= r;
  return pk(r.x, r.y);
}
#define MF(o, wv, h, c) "v_mfma_f32_16x16x32_bf16 " o ", " wv ", " h ", " c "\n\t"
#define CHK_A(n0, n1, n2, n3) "v_bitop3_b32 %[t0], " n0 ", " n1 ", %[S] bitop3:0x7e\n\tv_bitop3_b32 %[t1], " n2 ", " n3 ", %[S] bitop3:0x7e\n\t"
#define CHK_B "v_bitop3_b32 %[t0], %[t0], %[t1], %[M] bitop3:0xa8\n\tv_cmp_ne_u32 vcc, 0, %[t0]\n\t"
#define RETRY(n, n0, n1, n2, n3) \
  "s_cbranch_vccz 2f\n\t" \
  "1:\n\ts_cmp_eq_u32 %[cnt], 0\n\ts_cbranch_scc1 2f\n\ts_sub_u32 %[cnt], %[cnt], 1\n\t" \
  "buffer_load_dwordx4 " n ", %[vr], %[rw], %[sn] offen sc1\n\ts_waitcnt vmcnt(0)\n\t" \
  CHK_A(n0, n1, n2, n3) CHK_B "s_cbranch_vccnz 1b\n\t2:\n\t"
#define HR(a, b) "v[" #a ":" #b "]"
#define VR(a) "v" #a
#define STEP_MID(I, c0, c3, n0, n1, n2, n3, C0, C1, C2, C3, OC) \
  asm volatile(MF("%[o0]", "%[w0]", HR(c0, c3), C0) "s_waitcnt vmcnt(6)\n\t" MF("%[o1]", "%[w1]", HR(c0, c3), C1) \
      CHK_A(VR(n0), VR(n1), VR(n2), VR(n3)) MF("%[o2]", "%[w2]", HR(c0, c3), C2) CHK_B \
      RETRY(HR(n0, n3), VR(n0), VR(n1), VR(n2), VR(n3)) MF("%[o3]", "%[w3]", HR(c0, c3), C3) \
      "buffer_load_dwordx4 " HR(c0, c3) ", %[vn], %[rw], %[sc] offen sc1" \
      : [o0] OC(o[0]), [o1] OC(o[1]), [o2] OC(o[2]), [o3] OC(o[3]), [t0] "=&v"(t0), [t1] "=&v"(t1), [cnt] "+s"(cnt), \
        "+{" HR(c0, c3) "}"(H[I]), "+{" HR(n0, n3) "}"(H[I + 1]) \
      : [w0] "v"(W[I][0]), [w1] "v"(W[I][1]), [w2] "v"(W[I][2]), [w3] "v"(W[I][3]), [S] "s"(S), [M] "s"(0x80008000u), \
        [vr] "v"(vo), [vn] "v"(vn), [rw] "s"(sws), [sn] "s"(so[I + 1]), [sc] "s"(so[I]) \
      : "memory", "vcc", "scc")
#define ACC "%[o0]", "%[o1]", "%[o2]", "%[o3]", "+v"
#define STEP_X(...) STEP_MID(__VA_ARGS__)
#define HLOAD(I, c0, c3) \
  asm volatile("buffer_load_dwordx4 " HR(c0, c3) ", %1, %2, %3 offen sc1" : "={" HR(c0, c3) "}"(H[I]) : "v"(vo), "s"(sws), "s"(so[I]) : "memory")
__device__ __forceinline__ bool any_lane(bool c) { return __builtin_amdgcn_ballot_w64(c) != 0; }

__global__ void __launch_bounds__(512, 2)
ffn_kernel(const u16* __restrict__ x, const u16* __restrict__ w1, const u16* __restrict__ b1,
           const u16* __restrict__ w2, const u16* __restrict__ b2, u16* __restrict__ out, char* ws) {
  extern __shared__ __attribute__((aligned(16))) char lds_[];
  const lptr L = (lptr)lds_;
  const int b = blockIdx.x;
  const int s = (b & 7) >> 1, idx = (b & 1) * 32 + (b >> 3);
  if (idx >= 63) return;
  const int mb = idx / 9, g = idx - mb * 9;
  const int mt0 = mb < 4 ? 5 * mb : 4 * mb + 4, nm = mb < 4 ? 5 : 4;
  const int wave = __builtin_amdgcn_readfirstlane(threadIdx.x >> 6), lane = threadIdx.x & 63;
  STAMP(0);
  asm volatile("" :: "s"(x), "s"(w1), "s"(w2), "s"(ws), "s"(b1));
  const __amdgpu_buffer_rsrc_t rw = __builtin_amdgcn_make_buffer_rsrc(ws, 0, WS_END, 0x00020000);
  const int q = lane >> 4;

  if (wave >= 4) {
    const int u = wave - 4;
    if (u < 2) {
      __builtin_amdgcn_s_setprio(3);
      const i32x4 sx = make_srd(x, (u32)(mt0 + nm) * 16u * 1152u), sw1 = make_srd(w1, 576u * 9216u), sw2 = make_srd(w2, 4608u * 1152u);
      const u32 par = (u32)u;
      const u32 vx = (u32)(lane >> 3) * 1152u + (u32)((lane & 7) ^ (4 * par + (lane >> 4))) * 16u;
      const u32 vw = (u32)(lane >> 4) * 9216u + (u32)(((lane & 15) >> 1) ^ ((lane >> 4) | (par << 2))) * 32u + (u32)(lane & 1) * 16u;
      const u32 v2 = (u32)(lane >> 3) * 1152u + (u32)(((lane & 7) >> 1) ^ ((lane >> 4) & 3)) * 32u + (u32)(lane & 1) * 16u;
      const u32 oxb = (u32)(mt0 * 16 + 8 * par) * 1152u, owb = par * 8u * 9216u + (u32)(s * 9 + g) * 256u;
      const u32 o2b = (u32)(s * 1152 + 8 * par) * 1152u + (u32)g * 128u;
      const u32 mxb = par * 1024u, mwb = 10240u + par * 2048u;
      u32 kp = ROT ? (u32)(mb * 4 / 3) : 0u, slot = 0;
      auto st1 = [&]() {
        issue1(sx, sw1, vx, vw, oxb + kp * 128u, owb + kp * (64u * 9216u), mxb + slot, mwb + slot);
        kp = kp == 8u ? 0u : kp + 1u; slot = slot == 5u * SLOT ? 0u : slot + SLOT;
      };
      u32 gp = (u32)g;
      auto st2 = [&]() {
        issue2(sw2, v2, o2b + gp * (128u * 1152u), mxb + slot);
        gp = gp == 8u ? 0u : gp + 1u; slot = slot == 5u * SLOT ? 0u : slot + SLOT;
      };
      st1(); st1();
      STAMP(1);
#pragma unroll 1
      for (int j = 0; j < 7; ++j) { WAITV_BAR(13); st1(); }
      WAITV_BAR(13); st2(); st2();
      STAMP(2);
#pragma unroll 1
      for (int j = 8; j < 15; ++j) { WAITV_BAR(16); st2(); }
      WAITV_BAR(16); WAITV_BAR(8); WAITV_BAR(0);
      STAMP(3);
      __builtin_amdgcn_s_setprio(0);
    } else {
      const u32 Ev = __builtin_amdgcn_raw_buffer_load_b32(rw, 0, mb * 256, 16);
      if (lane < 20) *(AS3 u32*)(L + LDS_FLAGS + lane * 4) = 0u;
      BAR_L();
#pragma unroll 1
      for (int j = 1; j < 7; ++j) BAR();
      *(AS3 u32*)(L + LDS_FLAGS + 128) = ~Ev;
      BAR_L();
#pragma unroll 1
      for (int j = 8; j < 18; ++j) BAR();
    }
    const u32 T = __builtin_amdgcn_readfirstlane(*(AS3 u32*)(L + LDS_FLAGS + 128));
    const u32 sgn = (T << 16) & 0x80000000u, nib = T & 15u;
    u32x2 bb = {0u, 0u};
    if (u == s) bb = *(const u32x2*)(b2 + g * 64 + s * 16 + q * 4);
    const u32 pout = WS_P + (u32)(((mb * 9 + g) * 4 + u) * 4 + s) * 5120u;
#pragma unroll 1
    for (int mt = 0; mt < nm; ++mt) {
#pragma unroll 1
      for (int it = 0; it < (SPIN_MAX << 6); ++it) {
        const u32x4 f = *(volatile AS3 u32x4*)(L + LDS_FLAGS + mt * 16);
        if (__builtin_amdgcn_readfirstlane(f.x & f.y & f.z & f.w) == 1u) break;
        __builtin_amdgcn_s_sleep(1);
      }
      asm volatile("" ::: "memory");
      const lptr r = L + mt * SLOT + u * 1024 + lane * 16;
      f32x4 v = *(AS3 f32x4*)r;
      v += *(AS3 f32x4*)(r + 4096); v += *(AS3 f32x4*)(r + 8192); v += *(AS3 f32x4*)(r + 12288);
      u32x4 w = __builtin_bit_cast(u32x4, v) ^ sgn;
      if (u == s) *(AS3 u32x4*)r = w;
      else __builtin_amdgcn_raw_buffer_store_b128((w & ~15u) | nib, rw, lane * 16, pout + mt * 1024, 16);
      STAMP(4 + mt);
    }
    if (u == s) {
      const u32 pin = WS_P + (u32)((mb * 9 + g) * 4 + s) * 4 * 5120u;
      u32 so[3];
#pragma unroll
      for (int k = 0; k < 3; ++k) so[k] = pin + (u32)(k + (k >= s ? 1 : 0)) * 5120u;
      u32x4 p[5][3];
#pragma unroll
      for (int mt = 0; mt < 5; ++mt) {
        const u32 vo = (mt < 4 || nm == 5) ? (u32)lane * 16u + mt * 1024u : 0x80000000u;
#pragma unroll
        for (int k = 0; k < 3; ++k) p[mt][k] = __builtin_amdgcn_raw_buffer_load_b128(rw, vo, so[k], 16);
      }
#pragma unroll
      for (int mt = 0; mt < 5; ++mt) {
        if (mt < nm) {
#pragma unroll 1
          for (int it = 0; it < SPIN_MAX; ++it) {
            u32 bad = 0;
#pragma unroll
            for (int k = 0; k < 3; ++k) bad |= (p[mt][k].x ^ nib) | (p[mt][k].y ^ nib) | (p[mt][k].z ^ nib) | (p[mt][k].w ^ nib);
            if (!any_lane((bad & 15u) != 0u)) break;
            asm volatile("" ::: "memory");
#pragma unroll
            for (int k = 0; k < 3; ++k) p[mt][k] = __builtin_amdgcn_raw_buffer_load_b128(rw, lane * 16 + mt * 1024, so[k], 16);
          }
          f32x4 v = *(AS3 f32x4*)(L + mt * SLOT + u * 1024 + lane * 16);
#pragma unroll
          for (int k = 0; k < 3; ++k) v += __builtin_bit_cast(f32x4, p[mt][k] & ~15u);
          const u32 p0 = pk(v.x, v.y), p1 = pk(v.z, v.w);
          u32x2 o;
          o.x = pk(lo(p0) + lo(bb.x), hi(p0) + hi(bb.x));
          o.y = pk(lo(p1) + lo(bb.y), hi(p1) + hi(bb.y));
          *(u32x2*)(out + (size_t)((mt0 + mt) * 16 + (lane & 15)) * 576 + g * 64 + s * 16 + q * 4) = o;
        }
      }
      if (s == 0 && g == 0 && lane == 0) __builtin_amdgcn_raw_buffer_store_b32(T, rw, 0, mb * 256, 16);
      STAMP(10);
    }
    return;
  }

  const int w = wave;
  const int a = (lane >> 2) & 3, p = lane & 3, m = lane & 15;
  const u32x2 bA = *(const u32x2*)(b1 + (s * 9 + g) * 128 + w * 32 + q * 4);
  const u32x2 bB = *(const u32x2*)(b1 + (s * 9 + g) * 128 + w * 32 + 16 + q * 4);
  const u32 Ev = __builtin_amdgcn_raw_buffer_load_b32(rw, 0, mb * 256, 16);
  f32x4 acc[2][5];
#pragma unroll
  for (int t = 0; t < 2; ++t)
#pragma unroll
    for (int mt = 0; mt < 5; ++mt) acc[t][mt] = (f32x4){0.f, 0.f, 0.f, 0.f};
  {
    const u32 xa0 = (u32)m * 128u + (u32)(q ^ (m >> 1)) * 16u;
    const u32 wa0 = 10240u + (u32)(8 * q + a) * 256u + (u32)((2 * w) ^ (a | ((q & 1) << 2))) * 32u + (u32)p * 8u;
    u32 slot = 0;
#pragma unroll 1
    for (int j = 0; j < 9; ++j) {
      BAR();
      bf16x8 A[2][2], B[2][5];
#pragma unroll
      for (int ks = 0; ks < 2; ++ks) {
#pragma unroll
        for (int t = 0; t < 2; ++t) {
          const lptr pw = L + (slot + (wa0 ^ (t * 32u))) + ks * 8192;
          A[ks][t] = tr2(pw, pw + 1024);
        }
#pragma unroll
        for (int mt = 0; mt < 5; ++mt) B[ks][mt] = *(AS3 bf16x8*)(L + (slot + (xa0 ^ (ks * 64u))) + mt * 2048);
        __builtin_amdgcn_sched_barrier(0);
      }
#pragma unroll
      for (int ks = 0; ks < 2; ++ks)
#pragma unroll
        for (int mt = 0; mt < 5; ++mt)
#pragma unroll
          for (int t = 0; t < 2; ++t) acc[t][mt] = __builtin_amdgcn_mfma_f32_16x16x32_bf16(A[ks][t], B[ks][mt], acc[t][mt], 0, 0, 0);
      slot = slot == 5u * SLOT ? 0u : slot + SLOT;
    }
  }
  STAMP(1);
  const u32 T = ~__builtin_amdgcn_readfirstlane(Ev);
  const i32x4 sws = make_srd(ws, WS_END);
  const f32x2 bv[4] = {{lo(bA.x), hi(bA.x)}, {lo(bA.y), hi(bA.y)}, {lo(bB.x), hi(bB.x)}, {lo(bB.y), hi(bB.y)}};
  u32 vo = (u32)lane * 16u;
  const u32 S = ((T >> 15) & 1u) * 0x80008000u;
  const u32 hb = WS_H + (u32)(mb * 4 + s) * (5u * 36864u) + (u32)w * 1024u;
  u32 so[9];
#pragma unroll
  for (int i = 0; i < 9; ++i) so[i] = hb + (u32)(g + i >= 9 ? g + i - 9 : g + i) * 4096u;
  bf16x8 W[9][4];
  u32x4 H[9];
  const u32 wb = (u32)(32 * w + 4 * q + a) * 128u + (u32)((2 * q + (a >> 1)) & 3) * 32u + (u32)p * 8u;
#pragma unroll
  for (int i = 0; i < 9; ++i) {
    if (i < 5) {
      u32x4 h;
      h.x = act2(acc[0][i].x, acc[0][i].y, bv[0]) | S; h.y = act2(acc[0][i].z, acc[0][i].w, bv[1]) | S;
      h.z = act2(acc[1][i].x, acc[1][i].y, bv[2]) | S; h.w = act2(acc[1][i].z, acc[1][i].w, bv[3]) | S;
      if (i < 4 || nm == 5) __builtin_amdgcn_raw_buffer_store_b128(h, rw, lane * 16, so[0] + i * 36864u, 16);
      const int k = i * 4 + w;
      *(AS3 u32x4*)(L + (3 * SLOT + 16384 + k * 1024 + (k >= 10 ? 16384 : 0)) + lane * 16) = h;
      if (i == 4) STAMP(2);
    }
    BAR_L();
#pragma unroll
    for (int t = 0; t < 4; ++t) {
      const lptr pw = L + (wb ^ (t * 32u)) + ((9 + i) % 6) * SLOT;
      W[i][t] = tr2(pw, pw + 2048);
    }
    if (i == HPRE) {
      asm volatile("s_nop 4");
      HLOAD(1, 224, 227); HLOAD(2, 228, 231); HLOAD(3, 232, 235); HLOAD(4, 236, 239);
      HLOAD(5, 240, 243); HLOAD(6, 244, 247); HLOAD(7, 248, 251); HLOAD(8, 252, 255);
    }
    __builtin_amdgcn_sched_barrier(0);
  }
  STAMP(3);
  u32 cnt = SPIN_MAX, t0, t1;
  asm volatile("s_waitcnt vmcnt(7)\n\t" CHK_A("v224", "v225", "v226", "v227") CHK_B RETRY("v[224:227]", "v224", "v225", "v226", "v227")
               : [t0] "=&v"(t0), [t1] "=&v"(t1), [cnt] "+s"(cnt), "+{v[224:227]}"(H[1])
               : [S] "s"(S), [M] "s"(0x80008000u), [vr] "v"(vo), [rw] "s"(sws), [sn] "s"(so[1]) : "memory", "vcc", "scc");
#pragma unroll 1
  for (int mt = 0; mt < nm; ++mt) {
    const int k = mt * 4 + w;
    H[0] = *(AS3 u32x4*)(L + (3 * SLOT + 16384 + k * 1024 + (k >= 10 ? 16384 : 0)) + lane * 16);
    const u32 vn = mt + 1 < nm ? vo + 36864u : 0x80000000u;
    const u32 mn = mt + 1 < nm ? 0x80008000u : 0u;
    f32x4 o[4];
    STEP_X(1, 224, 227, 228, 229, 230, 231, "0", "0", "0", "0", "=&v");
    STEP_X(2, 228, 231, 232, 233, 234, 235, ACC);
    STEP_X(3, 232, 235, 236, 237, 238, 239, ACC);
    STEP_X(4, 236, 239, 240, 241, 242, 243, ACC);
    STEP_X(5, 240, 243, 244, 245, 246, 247, ACC);
    STEP_X(6, 244, 247, 248, 249, 250, 251, ACC);
    STEP_X(7, 248, 251, 252, 253, 254, 255, ACC);
    asm volatile(MF("%[o0]", "%[w0]", "v[252:255]", "%[o0]") MF("%[o1]", "%[w1]", "v[252:255]", "%[o1]")
                 MF("%[o2]", "%[w2]", "v[252:255]", "%[o2]") MF("%[o3]", "%[w3]", "v[252:255]", "%[o3]")
                 "buffer_load_dwordx4 v[252:255], %[vn], %[rw], %[sc] offen sc1"
                 : [o0] "+v"(o[0]), [o1] "+v"(o[1]), [o2] "+v"(o[2]), [o3] "+v"(o[3]), "+{v[252:255]}"(H[8])
                 : [w0] "v"(W[8][0]), [w1] "v"(W[8][1]), [w2] "v"(W[8][2]), [w3] "v"(W[8][3]), [vn] "v"(vn), [rw] "s"(sws), [sc] "s"(so[8])
                 : "memory");
    asm volatile(MF("%[o0]", "%[w0]", "%[h]", "%[o0]") "s_waitcnt vmcnt(7)\n\t" MF("%[o1]", "%[w1]", "%[h]", "%[o1]")
                 CHK_A("v224", "v225", "v226", "v227") MF("%[o2]", "%[w2]", "%[h]", "%[o2]") CHK_B
                 RETRY("v[224:227]", "v224", "v225", "v226", "v227") MF("%[o3]", "%[w3]", "%[h]", "%[o3]") "s_nop 7"
                 : [o0] "+v"(o[0]), [o1] "+v"(o[1]), [o2] "+v"(o[2]), [o3] "+v"(o[3]), [t0] "=&v"(t0), [t1] "=&v"(t1),
                   [cnt] "+s"(cnt), "+{v[224:227]}"(H[1])
                 : [w0] "v"(W[0][0]), [w1] "v"(W[0][1]), [w2] "v"(W[0][2]), [w3] "v"(W[0][3]), [h] "v"(H[0]), [S] "s"(S),
                   [M] "s"(mn), [vr] "v"(vn), [rw] "s"(sws), [sn] "s"(so[1])
                 : "memory", "vcc", "scc");
    const lptr r = L + mt * SLOT + w * 4096 + lane * 16;
#pragma unroll
    for (int t = 0; t < 4; ++t) *(AS3 f32x4*)(r + t * 1024) = o[t];
    asm volatile("" ::: "memory");
    if (lane == 0) *(volatile AS3 u32*)(L + LDS_FLAGS + mt * 16 + w * 4) = 1u;
    vo = vn;
    STAMP(4 + mt);
  }
}

__global__ void __launch_bounds__(64) padk() {
  u64 t0, t_; asm volatile("s_memrealtime %0\n\ts_waitcnt lgkmcnt(0)" : "=s"(t0) :: "memory");

  do { asm volatile("s_sleep 1\n\ts_memrealtime %0\n\ts_waitcnt lgkmcnt(0)" : "=s"(t_) :: "memory"); } while (t_ < t0 + 3000u);
}
extern "C" void kernel_launch(void* const* d_in, const int* in_sizes, int n_in, void* d_out, int out_size, void* d_ws,
                              size_t ws_size, hipStream_t stream) {
  (void)in_sizes; (void)n_in; (void)out_size; (void)ws_size;
  (void)hipFuncSetAttribute((const void*)ffn_kernel, hipFuncAttributeMaxDynamicSharedMemorySize, LDS_BYTES);
  ffn_kernel<<<256, 512, LDS_BYTES, stream>>>((const u16*)d_in[0], (const u16*)d_in[1], (const u16*)d_in[2],
                                              (const u16*)d_in[3], (const u16*)d_in[4], (u16*)d_out, (char*)d_ws);
  (void)hipFuncSetAttribute((const void*)padk, hipFuncAttributeMaxDynamicSharedMemorySize, 1024);
  padk<<<1, 64, 0, stream>>>();
}
```

```cpp
#include <hip/hip_runtime.h>

typedef unsigned short u16;
typedef unsigned int u32;
typedef unsigned long long u64;
typedef __bf16 bf16x8 __attribute__((ext_vector_type(8)));
typedef __bf16 bf16x2 __attribute__((ext_vector_type(2)));
typedef short s16x4 __attribute__((ext_vector_type(4)));
typedef short s16x8 __attribute__((ext_vector_type(8)));
typedef float f32x4 __attribute__((ext_vector_type(4)));
typedef float f32x2 __attribute__((ext_vector_type(2)));
typedef u32 u32x4 __attribute__((ext_vector_type(4)));
typedef u32 u32x2 __attribute__((ext_vector_type(2)));
typedef int i32x4 __attribute__((ext_vector_type(4)));
#define AS3 __attribute__((address_space(3)))
#define AS4 __attribute__((address_space(4)))
typedef AS3 char* lptr;

constexpr int SLOT = 26 * 1024;
constexpr int LDS_FLAGS = 158 * 1024;
constexpr int LDS_BYTES = LDS_FLAGS + 256;
constexpr int F_PART = 0;
constexpr int F_W2 = 80;
constexpr int F_P1 = 96;
constexpr int F_ACK = 112;
constexpr int F_T = 128;
__host__ __device__ constexpr int w2area(int i) { return i < 5 ? (78 + 16 * i) * 1024 : (i - 5) * 16384; }
constexpr u32 WS_H = 4096;
constexpr u32 WS_P = WS_H + 7u * 4 * 5 * 36 * 1024;
constexpr u32 WS_END = WS_P + 7u * 9 * 16 * 5 * 1024;
constexpr int SPIN_MAX = 1 << 16;
#define OUT_AUX 17
#define PF_X 1
#define PF_W2 1
#define PF_CODE 1
#define PF_H 1
#define PF_TLB 1
#define POLL_GAP 4
#define W1SLEEP 1
#define CPRIO 2
#define WQ0 2
#define WQ1 2
#define WQ3 3
#define DEPTH 2

#define STAMP(k) do { } while (0)
#define STAMPR(k) do { } while (0)
#define FLUSH() do { } while (0)

__device__ __forceinline__ i32x4 make_srd(const void* p, u32 bytes) {
  u64 a = (u64)p;
  i32x4 r; r.x = (int)(u32)a; r.y = (int)(u32)(a >> 32) & 0xffff; r.z = (int)bytes; r.w = 0x00020000;
  return r;
}
#define BAR() asm volatile("s_barrier" ::: "memory")
#define BAR_L() asm volatile("s_waitcnt lgkmcnt(0)\n\ts_barrier" ::: "memory")
#define WAITV_BAR(n) asm volatile("s_waitcnt vmcnt(" #n ")\n\ts_barrier" ::: "memory")

#define DMA(v, s, o) "buffer_load_dwordx4 " v ", " s ", " o " offen lds\n\t"
#define STEP(o, ms, os) "s_add_u32 m0, m0, " #ms "\n\ts_add_u32 " o ", " o ", " #os "\n\t"
__device__ __forceinline__ void issue1(i32x4 sx, i32x4 sw, u32 vx, u32 vw, u32 ox, u32 ow, u32 mx, u32 mw) {
  asm volatile(
      "s_nop 4\n\t"
      "s_mov_b32 m0, %7\n\ts_nop 0\n\t"
      DMA("%3", "%5", "%1") STEP("%1", 1024, 36864) DMA("%3", "%5", "%1") STEP("%1", 3072, 110592)
      DMA("%3", "%5", "%1") STEP("%1", 1024, 36864) DMA("%3", "%5", "%1") STEP("%1", 3072, 110592)
      DMA("%3", "%5", "%1") STEP("%1", 1024, 36864) DMA("%3", "%5", "%1") STEP("%1", 3072, 110592)
      DMA("%3", "%5", "%1") STEP("%1", 1024, 36864) DMA("%3", "%5", "%1")
      "s_mov_b32 m0, %6\n\ts_nop 0\n\t"
      DMA("%2", "%4", "%0") STEP("%0", 2048, 18432) DMA("%2", "%4", "%0") STEP("%0", 2048, 18432)
      DMA("%2", "%4", "%0") STEP("%0", 2048, 18432) DMA("%2", "%4", "%0") STEP("%0", 2048, 18432)
      DMA("%2", "%4", "%0")
      : "+s"(ox), "+s"(ow) : "v"(vx), "v"(vw), "s"(sx), "s"(sw), "s"(mx), "s"(mw) : "memory", "scc");
}
__device__ __forceinline__ void issue2(i32x4 sw, u32 v, u32 o, u32 m) {
  asm volatile(
      "s_nop 4\n\t"
      "s_mov_b32 m0, %3\n\ts_nop 0\n\t"
      DMA("%1", "%2", "%0") STEP("%0", 2048, 18432) DMA("%1", "%2", "%0") STEP("%0", 2048, 18432)
      DMA("%1", "%2", "%0") STEP("%0", 2048, 18432) DMA("%1", "%2", "%0") STEP("%0", 2048, 18432)
      DMA("%1", "%2", "%0") STEP("%0", 2048, 18432) DMA("%1", "%2", "%0") STEP("%0", 2048, 18432)
      DMA("%1", "%2", "%0") STEP("%0", 2048, 18432) DMA("%1", "%2", "%0")
      : "+s"(o) : "v"(v), "s"(sw), "s"(m) : "memory", "scc");
}
__device__ __forceinline__ bf16x8 tr2(lptr p0, lptr p1) {
  s16x4 a = __builtin_amdgcn_ds_read_tr16_b64_v4i16((AS3 s16x4*)p0);
  s16x4 b = __builtin_amdgcn_ds_read_tr16_b64_v4i16((AS3 s16x4*)p1);
  s16x8 c = __builtin_shufflevector(a, b, 0, 1, 2, 3, 4, 5, 6, 7);
  return __builtin_bit_cast(bf16x8, c);
}
__device__ __forceinline__ u32 pk(float a, float b) {
  bf16x2 v = {(__bf16)a, (__bf16)b};
  return __builtin_bit_cast(u32, v);
}
__device__ __forceinline__ float lo(u32 p) { return __uint_as_float(p << 16); }
__device__ __forceinline__ float hi(u32 p) { return __uint_as_float(p & 0xffff0000u); }
__device__ __forceinline__ float relu(float v) {
  float r; asm("v_max_f32 %0, 0, %1" : "=v"(r) : "v"(v)); return r;
}
__device__ __forceinline__ u32 act2(float a0, float a1, f32x2 bv) {
  u32 p = pk(a0, a1);
  asm("" : "+v"(p));
  f32x2 v = {lo(p), hi(p)};
  v += bv;
  p = pk(v.x, v.y);
  asm("" : "+v"(p));
  f32x2 r = {relu(lo(p)), relu(hi(p))};
  r *= r;
  return pk(r.x, r.y);
}
#define WV6 "s_waitcnt vmcnt(6)\n\t"
#define MF(o, wv, h, c) "v_mfma_f32_16x16x32_bf16 " o ", " wv ", " h ", " c "\n\t"
#define HR(a, b) "v[" #a ":" #b "]"
#define X2(a, b) "v_xor_b32 v" #a ", %[S], v" #a "\n\tv_xor_b32 v" #b ", %[S], v" #b "\n\t"
#define O2(a, b, c, d) "v_or3_b32 %[t], %[t], v" #a ", v" #b "\n\tv_or3_b32 %[t], %[t], v" #c ", v" #d "\n\t"
#define RF(c0, c3) "buffer_load_dwordx4 " HR(c0, c3) ", %[vn], %[rw], %[sc] offen sc1\n\t"
#define DW(o, off) "ds_write_b128 %[ra], " o " offset:" #off "\n\t"
#define WOPS(I) [w0] "v"(W[I][0]), [w1] "v"(W[I][1]), [w2] "v"(W[I][2]), [w3] "v"(W[I][3])
#define STEP_MID(I, c0, c3, n0, n1, n2, n3, C0, C1, C2, C3, OC) \
  asm volatile(MF("%[o0]", "%[w0]", HR(c0, c3), C0) WV6 X2(n0, n1) MF("%[o1]", "%[w1]", HR(c0, c3), C1) \
      X2(n2, n3) MF("%[o2]", "%[w2]", HR(c0, c3), C2) O2(n0, n1, n2, n3) MF("%[o3]", "%[w3]", HR(c0, c3), C3) RF(c0, c3) \
      : [o0] OC(o[0]), [o1] OC(o[1]), [o2] OC(o[2]), [o3] OC(o[3]), [t] "+v"(t), \
        "+{" HR(c0, c3) "}"(H[I]), "+{" HR(n0, n3) "}"(H[I + 1]) \
      : WOPS(I), [S] "s"(S), [vn] "v"(vn), [rw] "s"(sws), [sc] "s"(so[I]) : "memory")
#define ACC "%[o0]", "%[o1]", "%[o2]", "%[o3]", "+v"
#define STEP_X(...) STEP_MID(__VA_ARGS__)
#define HLOAD(I, c0, c3) \
  asm volatile("buffer_load_dwordx4 " HR(c0, c3) ", %1, %2, %3 offen sc1" : "={" HR(c0, c3) "}"(H[I]) : "v"(vo), "s"(sws), "s"(so[I]) : "memory")
__device__ __forceinline__ bool any_lane(bool c) { return __builtin_amdgcn_ballot_w64(c) != 0; }

#define LDSW(off, v) (*(volatile AS3 u32*)(L + LDS_FLAGS + (off)) = (v))
__device__ __forceinline__ void touch(i32x4 srd, u32 voff, u32 soff) {
  asm volatile("buffer_load_dword v255, %0, %1, %2 offen" :: "v"(voff), "s"(srd), "s"(soff) : "memory", "v255");
}
__device__ __forceinline__ void touch1(i32x4 srd, u32 voff, u32 soff) {
  asm volatile("buffer_load_dword v255, %0, %1, %2 offen sc1" :: "v"(voff), "s"(srd), "s"(soff) : "memory", "v255");
}
__device__ __forceinline__ u32 min4(u32x4 f) { return min(min(f.x, f.y), min(f.z, f.w)); }
__device__ __forceinline__ void wait4(lptr p, u32 need) {
#pragma unroll 1
  for (int it = 0; it < (SPIN_MAX << 6); ++it) {
    const u32x4 f = *(volatile AS3 u32x4*)p;
    if (__builtin_amdgcn_readfirstlane(min4(f)) >= need) break;
    __builtin_amdgcn_s_sleep(1);
  }
  asm volatile("" ::: "memory");
}
__device__ __forceinline__ void wait2(lptr p, u32 need, u32& have) {
  if (__builtin_expect(have < need, 0)) {
#pragma unroll 1
    for (int it = 0; have < need && it < (SPIN_MAX << 6); ++it) {
      const u32x2 f = *(volatile AS3 u32x2*)p;
      have = __builtin_amdgcn_readfirstlane(min(f.x, f.y));
    }
  }
  asm volatile("" ::: "memory");
}
__device__ __forceinline__ void wait1(lptr p, u32 need) {
#pragma unroll 1
  for (int it = 0; it < (SPIN_MAX << 6); ++it) {
    if (__builtin_amdgcn_readfirstlane(*(volatile AS3 u32*)p) >= need) break;
    __builtin_amdgcn_s_sleep(W1SLEEP);
  }
  asm volatile("" ::: "memory");
}
#define WAITV(n) asm volatile("s_waitcnt vmcnt(" #n ")" ::: "memory")

extern "C" __global__ void __launch_bounds__(512, 2)
ffn_kernel(const u16* __restrict__ x, const u16* __restrict__ w1, const u16* __restrict__ b1,
           const u16* __restrict__ w2, const u16* __restrict__ b2, u16* __restrict__ out, char* ws) {
  extern __shared__ __attribute__((aligned(16))) char lds_[];
  const lptr L = (lptr)lds_;
  const u32 b = blockIdx.x;
  const u32 s = (b & 7) >> 1, idx = (b & 1) * 32 + (b >> 3);
  if (idx >= 63) return;
  const u32 mb = (idx * 57) >> 9, g = idx - mb * 9;
  const u32 mtp = (u32)(0x201C18130F0A0500ull >> (mb * 8));
  const u32 mt0 = mtp & 255u, nm = ((mtp >> 8) & 255u) - mt0;
  const u32 rot = (0x4207420u >> (mb * 4)) & 15u;
  const int wave = __builtin_amdgcn_readfirstlane(threadIdx.x >> 6);
  const u32 lane = threadIdx.x & 63;
  STAMPR(12);
  STAMP(0);
  asm volatile("" :: "s"(x), "s"(w1), "s"(b1), "s"(w2), "s"(b2), "s"(out), "s"(ws));
  const __amdgpu_buffer_rsrc_t rw = __builtin_amdgcn_make_buffer_rsrc(ws, 0, WS_END, 0x00020000);
  const u32 q = lane >> 4;

  if (__builtin_expect(wave < 4, 1)) {
    const u32 u = (u32)wave;
    const i32x4 sx = make_srd(x, (mt0 + nm) * 16u * 1152u), sw2 = make_srd(w2, 4608u * 1152u);
    if (__builtin_expect(u < 2, 1)) {
      __builtin_amdgcn_s_setprio(3);
      const i32x4 sw1 = make_srd(w1, 576u * 9216u);
      const u32 par = u;
      const u32 vx = (lane >> 3) * 1152u + ((lane & 7) ^ (4 * par + (lane >> 4))) * 16u;
      const u32 vw = (lane >> 4) * 9216u + (((lane & 15) >> 1) ^ ((lane >> 4) | (par << 2))) * 32u + (lane & 1) * 16u;
      const u32 oxb = (mt0 * 16 + 8 * par) * 1152u, owb = par * 8u * 9216u + (s * 9 + g) * 256u;
      const u32 mxb = par * 1024u, mwb = 10240u + par * 2048u;
      u32 kp = rot, slot = 0;
      auto st1 = [&]() {
        issue1(sx, sw1, vx, vw, oxb + kp * 128u, owb + kp * (64u * 9216u), mxb + slot, mwb + slot);
        kp = kp == 8u ? 0u : kp + 1u; slot = slot == 5u * SLOT ? 0u : slot + SLOT;
      };
      STAMP(5);
      st1();
      STAMP(6);
      st1();
      STAMP(1);
      const u32 v2 = (lane >> 3) * 1152u + (((lane & 7) >> 1) ^ ((lane >> 4) & 3)) * 32u + (lane & 1) * 16u;
      const u32 o2b = (s * 1152 + 8 * par) * 1152u + g * 128u;
      u32 gp = g, area = (u32)w2area(0);
      auto st2 = [&]() {
        issue2(sw2, v2, o2b + gp * (128u * 1152u), mxb + area);
        gp = gp == 8u ? 0u : gp + 1u; area = area == (u32)w2area(4) ? 0u : area + 16384u;
      };
#pragma unroll 1
      for (int j = 0; j < 7; ++j) {
        WAITV_BAR(13); st1();
      }
      WAITV_BAR(13); st2(); st2();
      STAMP(2);
      WAITV_BAR(16); st2();
#pragma unroll 1
      for (u32 i = 0; i < 6; ++i) {
        WAITV(16); LDSW(F_W2 + par * 4, i + 1);
        if (i == 3) wait4(L + LDS_FLAGS + F_P1, 1u);
        st2();
      }
      WAITV(16); LDSW(F_W2 + par * 4, 7u);
      WAITV(8); LDSW(F_W2 + par * 4, 8u);
      WAITV(0); LDSW(F_W2 + par * 4, 9u);
      STAMP(3);
      __builtin_amdgcn_s_setprio(0);
    } else {
      const i32x4 sws = make_srd(ws, WS_END);
      if (u == 2u) {
        if (PF_X) {
          const u32 l = g * 63u + lane, st = (l * 205u) >> 14, row = l - st * 80u;
          u32 kc = rot + 2u + st; kc = kc >= 9u ? kc - 9u : kc;
          touch(sx, (lane < 63u && st < 7u) ? row * 1152u + kc * 128u : 0x80000000u, mt0 * 16u * 1152u);
        }
      } else {
        if (PF_CODE && (b >> 3) < 3u) {
          u64 pc; u32 off, len;
          asm volatile("s_getpc_b64 %0\n9:\n\ts_mov_b32 %1, 9b-ffn_kernel\n\ts_mov_b32 %2, .Lffn_tail-ffn_kernel" : "=s"(pc), "=s"(off), "=s"(len));
          touch(make_srd((const void*)(pc - off), len), lane * 128u, (b >> 3) * 8192u);
        }
        if (PF_TLB) {
          const u32 a0 = WS_H + (mb * 4 + s) * (5u * 36864u), a1 = WS_P + (mb * 9 + g) * 16u * 5120u;
          touch1(sws, lane == 0u ? a0 : lane == 1u ? a0 + 184316u : lane == 2u ? a1 : lane == 3u ? a1 + 81916u : 0x80000000u, 0u);
          touch(make_srd(out, 589824u), lane == 0u ? mt0 * 16u * 1152u : 0x80000000u, 0u);
        }
      }
      const u32 Ev = __builtin_amdgcn_raw_buffer_load_b32(rw, 0, mb * 256, 16);
      if (lane < 32u) LDSW(lane * 4, 0u);
      BAR_L();
      if (PF_W2) {
        const u32 part = mb == 3u ? 3u : (mb & 3u);
        const u32 o = (s * 1152u + part * 288u + (u - 2u) * 144u) * 1152u + g * 128u;
        touch(sw2, lane * 1152u, o); touch(sw2, lane * 1152u, o + 64u * 1152u);
        touch(sw2, lane < 16u ? lane * 1152u : 0x80000000u, o + 128u * 1152u);
      }
#pragma unroll 1
      for (int j = 1; j < 7; ++j) BAR();
      LDSW(F_T, ~Ev);
      BAR_L();
      BAR();
      if (PF_H) {
        wait4(L + LDS_FLAGS + F_ACK, u - 1u);
        const u32 hg = WS_H + (mb * 4 + s) * (5u * 36864u) + g * 4096u + (u - 2u) * (2u * 36864u);
        if (PF_H == 1) touch1(sws, 36864u + (lane >> 5) * 36864u + (lane & 31u) * 128u, hg);
        if (PF_H == 2) { touch1(sws, 36864u + lane * 64u, hg); touch1(sws, 2u * 36864u + lane * 64u, hg); }
        if (PF_H == 3) {
#pragma unroll
          for (u32 k = 0; k < 4; ++k) touch1(sws, 36864u + (k >> 1) * 36864u + (k & 1u) * 2048u + lane * 32u, hg);
        }
      }
    }
    const u32 T = __builtin_amdgcn_readfirstlane(*(volatile AS3 u32*)(L + LDS_FLAGS + F_T));
    const u32 nib = T & 15u;
    u32x2 bb = *(const u32x2*)(b2 + g * 64 + s * 16 + q * 4);
    asm volatile("" : "+v"(bb));
    auto lsum = [&](u32 mt, u32 t) {
      const lptr r = L + (mt < 3u ? (u32)w2area(2) + mt * 16384u : (mt - 3u) * 16384u) + t * 1024 + lane * 16;
      const f32x4 a0 = *(AS3 f32x4*)r, a1 = *(AS3 f32x4*)(r + 4096), a2 = *(AS3 f32x4*)(r + 8192), a3 = *(AS3 f32x4*)(r + 12288);
      return (a0 + a1) + (a2 + a3);
    };
    if (u != s) {
      const u32 pout = WS_P + (((mb * 9 + g) * 4 + u) * 4 + s) * 5120u;
#pragma unroll 1
      for (u32 mt = 0; mt < nm; ++mt) {
        wait1(L + LDS_FLAGS + F_PART + mt * 4, 0x01010101u);
        const u32x4 w = __builtin_bit_cast(u32x4, lsum(mt, u));
        __builtin_amdgcn_raw_buffer_store_b128((w & ~15u) | nib, rw, lane * 16, pout + mt * 1024, 16);
        if (mt == 0) STAMP(4);
      }
    }
    STAMP(8);
    const u32 pin = WS_P + ((mb * 9 + g) * 4 + s) * 4 * 5120u;
    u32 so[3];
#pragma unroll
    for (u32 k = 0; k < 3; ++k) so[k] = pin + ((s + 1u + k) & 3u) * 5120u;
    const __amdgpu_buffer_rsrc_t ro = __builtin_amdgcn_make_buffer_rsrc(out, 0, 589824u, 0x00020000);
    const u32 oo = (mt0 * 16u + (lane & 15u)) * 1152u + (g * 64u + s * 16u + q * 4u) * 2u;
    u32 mt = (u - s) & 3u;
    if (mt == 0u || mt + 1u < nm) {
#pragma unroll 1
      for (;;) {
        wait1(L + LDS_FLAGS + F_PART + mt * 4, 0x01010101u);
        const u32 vi = lane * 16 + mt * 1024;
        u32x4 p[3], r[3];
        auto ld = [&](u32x4* d) {
#pragma unroll
          for (int k = 0; k < 3; ++k) d[k] = __builtin_amdgcn_raw_buffer_load_b128(rw, vi, so[k], 16);
          asm volatile("" ::: "memory");
        };
        auto ok = [&](const u32x4* d) {
          u32 bad = 0;
#pragma unroll
          for (int k = 0; k < 3; ++k) bad |= (d[k].x ^ nib) | (d[k].y ^ nib) | (d[k].z ^ nib) | (d[k].w ^ nib);
          return !any_lane((bad & 15u) != 0u);
        };
        ld(p);
        f32x4 v = lsum(mt, s);
        asm volatile("" : "+v"(v));
        __builtin_amdgcn_s_sleep(POLL_GAP);
#pragma unroll 1
        for (int it = 0; it < SPIN_MAX; ++it) {
          ld(r);
          if (ok(p)) break;
          ld(p);
          if (ok(r)) { p[0] = r[0]; p[1] = r[1]; p[2] = r[2]; break; }
        }
        STAMP(11);
#pragma unroll
        for (int k = 0; k < 3; ++k) v += __builtin_bit_cast(f32x4, p[k] & ~15u);
        const u32 p0 = pk(v.x, v.y), p1 = pk(v.z, v.w);
        u32x2 o;
        o.x = pk(lo(p0) + lo(bb.x), hi(p0) + hi(bb.x));
        o.y = pk(lo(p1) + lo(bb.y), hi(p1) + hi(bb.y));
        __builtin_amdgcn_raw_buffer_store_b64(o, ro, oo, mt * (16 * 1152), OUT_AUX);
        if (mt != 0u) break;
        STAMP(9);
        mt = nm - 1u;
      }
      if (u == 0u && s == 0u && g == 0u && lane == 0u) __builtin_amdgcn_raw_buffer_store_b32(T, rw, 0, mb * 256, 16);
    }
    STAMP(10);
    FLUSH();
    return;
  }

  const u32 w = (u32)wave - 4u;
  if (CPRIO) __builtin_amdgcn_s_setprio(CPRIO);
  const u32 a = (lane >> 2) & 3, p = lane & 3, m = lane & 15;
  const u32x2 bA = *(const u32x2*)(b1 + (s * 9 + g) * 128 + w * 32 + q * 4);
  const u32x2 bB = *(const u32x2*)(b1 + (s * 9 + g) * 128 + w * 32 + 16 + q * 4);
  const u32 Ev = __builtin_amdgcn_raw_buffer_load_b32(rw, 0, mb * 256, 16);
  f32x4 acc[2][5];
#pragma unroll
  for (int t = 0; t < 2; ++t)
#pragma unroll
    for (int mt = 0; mt < 5; ++mt) acc[t][mt] = (f32x4){0.f, 0.f, 0.f, 0.f};
  {
    const u32 xa0 = m * 128u + (q ^ (m >> 1)) * 16u;
    const u32 wa0 = 10240u + (8 * q + a) * 256u + ((2 * w) ^ (a | ((q & 1) << 2))) * 32u + p * 8u;
    bf16x8 A[2][2], B[2][5];
    auto rd = [&](int ks, u32 slot) {
#pragma unroll
      for (int t = 0; t < 2; ++t) {
        const lptr pw = L + (slot + (wa0 ^ (t * 32u))) + ks * 8192;
        A[ks][t] = tr2(pw, pw + 1024);
      }
#pragma unroll
      for (int mt = 0; mt < 5; ++mt) B[ks][mt] = *(AS3 bf16x8*)(L + (slot + (xa0 ^ (ks * 64u))) + mt * 2048);
    };
    auto mm = [&](int ks) {
#pragma unroll
      for (int mt = 0; mt < 5; ++mt)
#pragma unroll
        for (int t = 0; t < 2; ++t) acc[t][mt] = __builtin_amdgcn_mfma_f32_16x16x32_bf16(A[ks][t], B[ks][mt], acc[t][mt], 0, 0, 0);
    };
    auto mix = [&]() {
#pragma unroll
      for (int k = 0; k < 2; ++k) { __builtin_amdgcn_sched_group_barrier(0x008, 1, 0); __builtin_amdgcn_sched_group_barrier(0x100, 2, 0); }
#pragma unroll
      for (int k = 0; k < 5; ++k) { __builtin_amdgcn_sched_group_barrier(0x008, 1, 0); __builtin_amdgcn_sched_group_barrier(0x100, 1, 0); }
      __builtin_amdgcn_sched_group_barrier(0x008, 3, 0);
    };
    u32 slot = 0;
    BAR();
    rd(0, 0u);
#pragma unroll 1
    for (u32 j = 0; j < 9; ++j) {
      __builtin_amdgcn_sched_barrier(0);
      rd(1, slot); mm(0); mix();
      __builtin_amdgcn_sched_barrier(0);
      if (j == 3) STAMP(15);
      asm volatile("s_cmp_eq_u32 %0, 8\n\ts_cbranch_scc1 1f\n\ts_barrier\n\t1:" :: "s"(j) : "memory", "scc");
      if (j == 3) STAMP(16);
      slot = slot == 5u * SLOT ? 0u : slot + SLOT;
      __builtin_amdgcn_sched_barrier(0);
      rd(0, slot); mm(1); mix();
      __builtin_amdgcn_sched_barrier(0);
      if (j == 3) STAMP(17);
      if (j == 2) STAMP(18);
    }
    __builtin_amdgcn_sched_barrier(0);
  }
  asm volatile("s_waitcnt lgkmcnt(0)" ::: "memory");
  LDSW(F_P1 + w * 4, 1u);
  STAMP(1);
  const u32 T = ~__builtin_amdgcn_readfirstlane(Ev);
  const i32x4 sws = make_srd(ws, WS_END);
  const f32x2 bv[4] = {{lo(bA.x), hi(bA.x)}, {lo(bA.y), hi(bA.y)}, {lo(bB.x), hi(bB.x)}, {lo(bB.y), hi(bB.y)}};
  u32 vo = lane * 16u;
  const u32 S = ((T >> 15) & 1u) * 0x80008000u;
  const u32 hb = WS_H + (mb * 4 + s) * (5u * 36864u) + w * 1024u;
  u32 so[9];
#pragma unroll
  for (u32 i = 0; i < 9; ++i) so[i] = hb + (g + i >= 9 ? g + i - 9 : g + i) * 4096u;
  bf16x8 W[9][4];
  u32x4 H[9];
  u32 have = 0;
  const u32 wb = (32 * w + 4 * q + a) * 128u + ((2 * q + (a >> 1)) & 3) * 32u + p * 8u;
  const u32 lw = w * 4096u + lane * 16u;
  auto rdw = [&](int i) {
    wait2(L + LDS_FLAGS + F_W2, (u32)i + 1u, have);
    const int base = i < 4 ? w2area(0) : i == 4 ? w2area(4) : 0;
#pragma unroll
    for (int t = 0; t < 4; ++t) {
      u32 pa = (wb ^ (t * 32u)) + base;
      asm("" : "+v"(pa));
      const lptr pw = L + pa + (w2area(i) - base);
      W[i][t] = tr2(pw, pw + 2048);
    }
  };
  u32x4 hp;
#pragma unroll
  for (int i = 0; i < 5; ++i) {
    u32x4 h;
    h.x = act2(acc[0][i].x, acc[0][i].y, bv[0]); h.y = act2(acc[0][i].z, acc[0][i].w, bv[1]);
    h.z = act2(acc[1][i].x, acc[1][i].y, bv[2]); h.w = act2(acc[1][i].z, acc[1][i].w, bv[3]);
    asm volatile("buffer_store_dwordx4 %0, %1, %2, %3 offen sc1\n\ts_nop 1" :: "v"(h | S), "v"((i < 4 || nm == 5u) ? vo + i * 36864u : 0x80000000u), "s"(sws), "s"(so[0]) : "memory");
    if (i == 0) { hp = h; for (int k = 0; k < WQ0; ++k) rdw(k); }
    if (i == 1) { for (int k = WQ0; k < WQ1; ++k) rdw(k); *(AS3 u32x4*)(L + w2area(0) + lw) = hp; }
    if (i == 3) { for (int k = WQ1; k < WQ3; ++k) rdw(k); }
    if (i >= 1) *(AS3 u32x4*)(L + (i < 4 ? w2area(0) + i * 1024 : w2area(1)) + lw) = h;
    __builtin_amdgcn_sched_barrier(0);
  }
  STAMP(2);
  HLOAD(1, 224, 227); HLOAD(2, 228, 231); HLOAD(3, 232, 235); HLOAD(4, 236, 239);
  HLOAD(5, 240, 243); HLOAD(6, 244, 247); HLOAD(7, 248, 251); HLOAD(8, 252, 255);
  if (PF_H) { WAITV(10); LDSW(F_ACK + w * 4, 1u); }
#pragma unroll
  for (int k = WQ3; k < 9; ++k) rdw(k);
  STAMP(3);
  u32 cnt = SPIN_MAX, t, first = 1, mt = 0;
  u32 ra = (u32)w2area(2) + lw, oh = (u32)w2area(0) + lw, fa = LDS_FLAGS + F_PART + w;
  u32 vn = vo + 36864u;
#define GC(a, b, c, d) "v_bitop3_b32 %[t1], v" #a ", v" #b ", %[S] bitop3:0x7e\n\tv_bitop3_b32 %[t2], v" #c ", v" #d ", %[S] bitop3:0x7e\n\tv_or3_b32 %[t], %[t], %[t1], %[t2]\n\t"
#define GL(a, b, k) "buffer_load_dwordx4 v[" #a ":" #b "], %[vr], %[rw], %[s" #k "] offen sc1\n\t"
#pragma unroll 1
  for (;;) {
    u32 t1, t2;
    asm volatile("s_cmp_eq_u32 %[first], 0\n\ts_cbranch_scc1 3f\n\t"
                 "1:\n\ts_waitcnt vmcnt(0)\n\tv_mov_b32 %[t], 0\n\t"
                 GC(224, 225, 226, 227) GC(228, 229, 230, 231) GC(232, 233, 234, 235) GC(236, 237, 238, 239)
                 GC(240, 241, 242, 243) GC(244, 245, 246, 247) GC(248, 249, 250, 251) GC(252, 253, 254, 255)
                 "v_and_b32 %[t], %[M], %[t]\n\tv_cmp_ne_u32 vcc, 0, %[t]\n\ts_cbranch_vccz 2f\n\t"
                 "s_cmp_eq_u32 %[cnt], 0\n\ts_cbranch_scc1 2f\n\t3:\n\ts_sub_u32 %[cnt], %[cnt], 1\n\t"
                 GL(224, 227, 1) GL(228, 231, 2) GL(232, 235, 3) GL(236, 239, 4) GL(240, 243, 5) GL(244, 247, 6) GL(248, 251, 7) GL(252, 255, 8)
                 "s_branch 1b\n\t2:\n\t" X2(224, 225) X2(226, 227) "s_nop 1"
                 : [t] "=&v"(t), [t1] "=&v"(t1), [t2] "=&v"(t2), [cnt] "+s"(cnt), "+{v[224:227]}"(H[1]), "+{v[228:231]}"(H[2]),
                   "+{v[232:235]}"(H[3]), "+{v[236:239]}"(H[4]), "+{v[240:243]}"(H[5]), "+{v[244:247]}"(H[6]), "+{v[248:251]}"(H[7]), "+{v[252:255]}"(H[8])
                 : [S] "s"(S), [M] "s"(0x80008000u), [vr] "v"(vo), [rw] "s"(sws), [s1] "s"(so[1]), [s2] "s"(so[2]), [s3] "s"(so[3]), [s4] "s"(so[4]),
                   [s5] "s"(so[5]), [s6] "s"(so[6]), [s7] "s"(so[7]), [s8] "s"(so[8]), [first] "s"(first)
                 : "memory", "vcc", "scc");
    if (PF_H) LDSW(F_ACK + w * 4, 2u);
    if (first) STAMP(5);
    first = 0;
    f32x4 o[4];
    STEP_X(1, 224, 227, 228, 229, 230, 231, "0", "0", "0", "0", "=&v");
    bool done = false;
#pragma unroll 1
    for (;;) {
      asm volatile("ds_read_b128 %0, %1" : "=v"(H[0]) : "v"(oh) : "memory");
      STEP_X(2, 228, 231, 232, 233, 234, 235, ACC);
      STEP_X(3, 232, 235, 236, 237, 238, 239, ACC);
      STEP_X(4, 236, 239, 240, 241, 242, 243, ACC);
      STEP_X(5, 240, 243, 244, 245, 246, 247, ACC);
      STEP_X(6, 244, 247, 248, 249, 250, 251, ACC);
      STEP_X(7, 248, 251, 252, 253, 254, 255, ACC);
      u64 bad;
      asm volatile(MF("%[o0]", "%[w0]", "v[252:255]", "%[o0]") "v_and_b32 %[t], %[M], %[t]\n\ts_waitcnt lgkmcnt(0)\n\t"
                   MF("%[o0]", "%[u0]", "%[h]", "%[o0]") "v_cmp_ne_u32 %[bad], 0, %[t]\n\t" WV6
                   MF("%[o1]", "%[w1]", "v[252:255]", "%[o1]") X2(224, 225)
                   MF("%[o1]", "%[u1]", "%[h]", "%[o1]") X2(226, 227)
                   MF("%[o2]", "%[w2]", "v[252:255]", "%[o2]") DW("%[o0]", 0)
                   MF("%[o2]", "%[u2]", "%[h]", "%[o2]") "v_or_b32 %[t], v224, v225\n\tv_or3_b32 %[t], %[t], v226, v227\n\t"
                   MF("%[o3]", "%[w3]", "v[252:255]", "%[o3]") RF(252, 255) DW("%[o1]", 1024)
                   MF("%[o3]", "%[u3]", "%[h]", "%[o3]")
                   : [o0] "+v"(o[0]), [o1] "+v"(o[1]), [o2] "+v"(o[2]), [o3] "+v"(o[3]), [t] "+v"(t), [bad] "=&s"(bad),
                     "+{v[252:255]}"(H[8]), "+{v[224:227]}"(H[1])
                   : WOPS(8), [u0] "v"(W[0][0]), [u1] "v"(W[0][1]), [u2] "v"(W[0][2]), [u3] "v"(W[0][3]), [h] "v"(H[0]),
                     [S] "s"(S), [M] "s"(0x80008000u), [vn] "v"(vn), [rw] "s"(sws), [sc] "s"(so[8]), [ra] "v"(ra)
                   : "memory");
      if (__builtin_expect(bad != 0ull && cnt != 0u, 0)) break;
      ++mt;
      vo = vn;
      vn = mt + 1u < nm ? vn + 36864u : 0x80000000u;
      asm volatile(MF("%[o0]", "%[w0]", "v[224:227]", "0") DW("%[o2]", 2048) WV6 X2(228, 229)
                   MF("%[o1]", "%[w1]", "v[224:227]", "0") X2(230, 231)
                   MF("%[o2]", "%[w2]", "v[224:227]", "0") DW("%[o3]", 3072) "ds_write_b8 %[fa], %[one]\n\t" O2(228, 229, 230, 231)
                   MF("%[o3]", "%[w3]", "v[224:227]", "0") RF(224, 227)
                   : [o0] "+v"(o[0]), [o1] "+v"(o[1]), [o2] "+v"(o[2]), [o3] "+v"(o[3]), [t] "+v"(t),
                     "+{v[224:227]}"(H[1]), "+{v[228:231]}"(H[2])
                   : WOPS(1), [S] "s"(S), [vn] "v"(vn), [rw] "s"(sws), [sc] "s"(so[1]), [ra] "v"(ra), [fa] "v"(fa), [one] "v"(1u)
                   : "memory");
      if (mt == 1u) STAMP(4);
      if (mt == nm) { done = true; break; }
      ra += mt == 3u ? 0u - (u32)w2area(4) : 16384u;
      oh += mt == 4u ? 16384u - 3072u : 1024u;
      fa += 4u;
    }
    if (done) break;
  }
  STAMP(8);
  asm volatile(".Lffn_tail:");
  FLUSH();
}

extern "C" void kernel_launch(void* const* d_in, const int* in_sizes, int n_in, void* d_out, int out_size, void* d_ws,
                              size_t ws_size, hipStream_t stream) {
  (void)in_sizes; (void)n_in; (void)out_size; (void)ws_size;
  (void)hipFuncSetAttribute((const void*)ffn_kernel, hipFuncAttributeMaxDynamicSharedMemorySize, LDS_BYTES);
  ffn_kernel<<<256, 512, LDS_BYTES, stream>>>((const u16*)d_in[0], (const u16*)d_in[1], (const u16*)d_in[2],
                                              (const u16*)d_in[3], (const u16*)d_in[4], (u16*)d_out, (char*)d_ws);
}
```

```cpp
#include <hip/hip_runtime.h>

typedef unsigned short u16;
typedef unsigned int u32;
typedef unsigned long long u64;
typedef __bf16 bf16x8 __attribute__((ext_vector_type(8)));
typedef __bf16 bf16x2 __attribute__((ext_vector_type(2)));
typedef short s16x4 __attribute__((ext_vector_type(4)));
typedef short s16x8 __attribute__((ext_vector_type(8)));
typedef float f32x4 __attribute__((ext_vector_type(4)));
typedef float f32x2 __attribute__((ext_vector_type(2)));
typedef u32 u32x4 __attribute__((ext_vector_type(4)));
typedef u32 u32x2 __attribute__((ext_vector_type(2)));
typedef int i32x4 __attribute__((ext_vector_type(4)));
#define AS3 __attribute__((address_space(3)))
#define AS4 __attribute__((address_space(4)))
typedef AS3 char* lptr;

constexpr int SLOT = 26 * 1024;
constexpr int LDS_FLAGS = 158 * 1024;
constexpr int LDS_BYTES = LDS_FLAGS + 256;
constexpr int F_PART = 0;
constexpr int F_W2 = 80;
constexpr int F_P1 = 96;
constexpr int F_ACK = 112;
constexpr int F_T = 128;
__host__ __device__ constexpr int w2area(int i) { return i < 5 ? (78 + 16 * i) * 1024 : (i - 5) * 16384; }
constexpr u32 WS_H = 4096;
constexpr u32 WS_P = WS_H + 7u * 4 * 5 * 36 * 1024;
constexpr u32 WS_END = WS_P + 7u * 9 * 16 * 5 * 1024;
constexpr int SPIN_MAX = 1 << 16;
#define OUT_AUX 17
#define PF_X 1
#define PF_W2 1
#define PF_CODE 1
#define PF_H 1
#define PF_TLB 1
#define POLL_GAP 4
#define W1SLEEP 1
#define CPRIO 2
#define WQ0 2
#define WQ1 2
#define WQ3 3
#define PGW 1
#define PAGE(l) ".p2align 12\n" l ":\n\ts_setpc_b64 vcc\n"
#define PGCALL asm volatile("s_swappc_b64 vcc, %0" :: "s"(pc + (u64)(long long)(int)(u == 2u ? o0 : o1)) : "vcc", "memory")
#define DEPTH 2

#define STAMP(k) do { } while (0)
#define STAMPR(k) do { } while (0)
#define FLUSH() do { } while (0)

__device__ __forceinline__ i32x4 make_srd(const void* p, u32 bytes) {
  u64 a = (u64)p;
  i32x4 r; r.x = (int)(u32)a; r.y = (int)(u32)(a >> 32) & 0xffff; r.z = (int)bytes; r.w = 0x00020000;
  return r;
}
#define BAR() asm volatile("s_barrier" ::: "memory")
#define BAR_L() asm volatile("s_waitcnt lgkmcnt(0)\n\ts_barrier" ::: "memory")
#define WAITV_BAR(n) asm volatile("s_waitcnt vmcnt(" #n ")\n\ts_barrier" ::: "memory")

#define DMA(v, s, o) "buffer_load_dwordx4 " v ", " s ", " o " offen lds\n\t"
#define STEP(o, ms, os) "s_add_u32 m0, m0, " #ms "\n\ts_add_u32 " o ", " o ", " #os "\n\t"
__device__ __forceinline__ void issue1(i32x4 sx, i32x4 sw, u32 vx, u32 vw, u32 ox, u32 ow, u32 mx, u32 mw) {
  asm volatile(
      "s_nop 4\n\t"
      "s_mov_b32 m0, %7\n\ts_nop 0\n\t"
      DMA("%3", "%5", "%1") STEP("%1", 1024, 36864) DMA("%3", "%5", "%1") STEP("%1", 3072, 110592)
      DMA("%3", "%5", "%1") STEP("%1", 1024, 36864) DMA("%3", "%5", "%1") STEP("%1", 3072, 110592)
      DMA("%3", "%5", "%1") STEP("%1", 1024, 36864) DMA("%3", "%5", "%1") STEP("%1", 3072, 110592)
      DMA("%3", "%5", "%1") STEP("%1", 1024, 36864) DMA("%3", "%5", "%1")
      "s_mov_b32 m0, %6\n\ts_nop 0\n\t"
      DMA("%2", "%4", "%0") STEP("%0", 2048, 18432) DMA("%2", "%4", "%0") STEP("%0", 2048, 18432)
      DMA("%2", "%4", "%0") STEP("%0", 2048, 18432) DMA("%2", "%4", "%0") STEP("%0", 2048, 18432)
      DMA("%2", "%4", "%0")
      : "+s"(ox), "+s"(ow) : "v"(vx), "v"(vw), "s"(sx), "s"(sw), "s"(mx), "s"(mw) : "memory", "scc");
}
__device__ __forceinline__ void issue2(i32x4 sw, u32 v, u32 o, u32 m) {
  asm volatile(
      "s_nop 4\n\t"
      "s_mov_b32 m0, %3\n\ts_nop 0\n\t"
      DMA("%1", "%2", "%0") STEP("%0", 2048, 18432) DMA("%1", "%2", "%0") STEP("%0", 2048, 18432)
      DMA("%1", "%2", "%0") STEP("%0", 2048, 18432) DMA("%1", "%2", "%0") STEP("%0", 2048, 18432)
      DMA("%1", "%2", "%0") STEP("%0", 2048, 18432) DMA("%1", "%2", "%0") STEP("%0", 2048, 18432)
      DMA("%1", "%2", "%0") STEP("%0", 2048, 18432) DMA("%1", "%2", "%0")
      : "+s"(o) : "v"(v), "s"(sw), "s"(m) : "memory", "scc");
}
__device__ __forceinline__ bf16x8 tr2(lptr p0, lptr p1) {
  s16x4 a = __builtin_amdgcn_ds_read_tr16_b64_v4i16((AS3 s16x4*)p0);
  s16x4 b = __builtin_amdgcn_ds_read_tr16_b64_v4i16((AS3 s16x4*)p1);
  s16x8 c = __builtin_shufflevector(a, b, 0, 1, 2, 3, 4, 5, 6, 7);
  return __builtin_bit_cast(bf16x8, c);
}
__device__ __forceinline__ u32 pk(float a, float b) {
  bf16x2 v = {(__bf16)a, (__bf16)b};
  return __builtin_bit_cast(u32, v);
}
__device__ __forceinline__ float lo(u32 p) { return __uint_as_float(p << 16); }
__device__ __forceinline__ float hi(u32 p) { return __uint_as_float(p & 0xffff0000u); }
__device__ __forceinline__ float relu(float v) {
  float r; asm("v_max_f32 %0, 0, %1" : "=v"(r) : "v"(v)); return r;
}
__device__ __forceinline__ u32 act2(float a0, float a1, f32x2 bv) {
  u32 p = pk(a0, a1);
  asm("" : "+v"(p));
  f32x2 v = {lo(p), hi(p)};
  v += bv;
  p = pk(v.x, v.y);
  asm("" : "+v"(p));
  f32x2 r = {relu(lo(p)), relu(hi(p))};
  r *= r;
  return pk(r.x, r.y);
}
#define WV6 "s_waitcnt vmcnt(6)\n\t"
#define MF(o, wv, h, c) "v_mfma_f32_16x16x32_bf16 " o ", " wv ", " h ", " c "\n\t"
#define HR(a, b) "v[" #a ":" #b "]"
#define X2(a, b) "v_xor_b32 v" #a ", %[S], v" #a "\n\tv_xor_b32 v" #b ", %[S], v" #b "\n\t"
#define O2(a, b, c, d) "v_or3_b32 %[t], %[t], v" #a ", v" #b "\n\tv_or3_b32 %[t], %[t], v" #c ", v" #d "\n\t"
#define RF(c0, c3) "buffer_load_dwordx4 " HR(c0, c3) ", %[vn], %[rw], %[sc] offen sc1\n\t"
#define DW(o, off) "ds_write_b128 %[ra], " o " offset:" #off "\n\t"
#define WOPS(I) [w0] "v"(W[I][0]), [w1] "v"(W[I][1]), [w2] "v"(W[I][2]), [w3] "v"(W[I][3])
#define STEP_MID(I, c0, c3, n0, n1, n2, n3, C0, C1, C2, C3, OC) \
  asm volatile(MF("%[o0]", "%[w0]", HR(c0, c3), C0) WV6 X2(n0, n1) MF("%[o1]", "%[w1]", HR(c0, c3), C1) \
      X2(n2, n3) MF("%[o2]", "%[w2]", HR(c0, c3), C2) O2(n0, n1, n2, n3) MF("%[o3]", "%[w3]", HR(c0, c3), C3) RF(c0, c3) \
      : [o0] OC(o[0]), [o1] OC(o[1]), [o2] OC(o[2]), [o3] OC(o[3]), [t] "+v"(t), \
        "+{" HR(c0, c3) "}"(H[I]), "+{" HR(n0, n3) "}"(H[I + 1]) \
      : WOPS(I), [S] "s"(S), [vn] "v"(vn), [rw] "s"(sws), [sc] "s"(so[I]) : "memory")
#define ACC "%[o0]", "%[o1]", "%[o2]", "%[o3]", "+v"
#define STEP_X(...) STEP_MID(__VA_ARGS__)
#define HLOAD(I, c0, c3) \
  asm volatile("buffer_load_dwordx4 " HR(c0, c3) ", %1, %2, %3 offen sc1" : "={" HR(c0, c3) "}"(H[I]) : "v"(vo), "s"(sws), "s"(so[I]) : "memory")
__device__ __forceinline__ bool any_lane(bool c) { return __builtin_amdgcn_ballot_w64(c) != 0; }

#define LDSW(off, v) (*(volatile AS3 u32*)(L + LDS_FLAGS + (off)) = (v))
__device__ __forceinline__ void touch(i32x4 srd, u32 voff, u32 soff) {
  asm volatile("buffer_load_dword v255, %0, %1, %2 offen" :: "v"(voff), "s"(srd), "s"(soff) : "memory", "v255");
}
__device__ __forceinline__ void touch1(i32x4 srd, u32 voff, u32 soff) {
  asm volatile("buffer_load_dword v255, %0, %1, %2 offen sc1" :: "v"(voff), "s"(srd), "s"(soff) : "memory", "v255");
}
__device__ __forceinline__ u32 min4(u32x4 f) { return min(min(f.x, f.y), min(f.z, f.w)); }
__device__ __forceinline__ void wait4(lptr p, u32 need) {
#pragma unroll 1
  for (int it = 0; it < (SPIN_MAX << 6); ++it) {
    const u32x4 f = *(volatile AS3 u32x4*)p;
    if (__builtin_amdgcn_readfirstlane(min4(f)) >= need) break;
    __builtin_amdgcn_s_sleep(1);
  }
  asm volatile("" ::: "memory");
}
__device__ __forceinline__ void wait2(lptr p, u32 need, u32& have) {
  if (__builtin_expect(have < need, 0)) {
#pragma unroll 1
    for (int it = 0; have < need && it < (SPIN_MAX << 6); ++it) {
      const u32x2 f = *(volatile AS3 u32x2*)p;
      have = __builtin_amdgcn_readfirstlane(min(f.x, f.y));
    }
  }
  asm volatile("" ::: "memory");
}
__device__ __forceinline__ void wait1(lptr p, u32 need) {
#pragma unroll 1
  for (int it = 0; it < (SPIN_MAX << 6); ++it) {
    if (__builtin_amdgcn_readfirstlane(*(volatile AS3 u32*)p) >= need) break;
    __builtin_amdgcn_s_sleep(W1SLEEP);
  }
  asm volatile("" ::: "memory");
}
#define WAITV(n) asm volatile("s_waitcnt vmcnt(" #n ")" ::: "memory")

extern "C" __global__ void __launch_bounds__(512, 2)
ffn_kernel(const u16* __restrict__ x, const u16* __restrict__ w1, const u16* __restrict__ b1,
           const u16* __restrict__ w2, const u16* __restrict__ b2, u16* __restrict__ out, char* ws) {
  extern __shared__ __attribute__((aligned(16))) char lds_[];
  const lptr L = (lptr)lds_;
  const u32 b = blockIdx.x;
  const u32 s = (b & 7) >> 1, idx = (b & 1) * 32 + (b >> 3);
  if (idx >= 63) return;
  const u32 mb = (idx * 57) >> 9, g = idx - mb * 9;
  const u32 mtp = (u32)(0x201C18130F0A0500ull >> (mb * 8));
  const u32 mt0 = mtp & 255u, nm = ((mtp >> 8) & 255u) - mt0;
  const u32 rot = (0x4207420u >> (mb * 4)) & 15u;
  const int wave = __builtin_amdgcn_readfirstlane(threadIdx.x >> 6);
  const u32 lane = threadIdx.x & 63;
  STAMPR(12);
  STAMP(0);
  asm volatile("" :: "s"(x), "s"(w1), "s"(b1), "s"(w2), "s"(b2), "s"(out), "s"(ws));
  const __amdgpu_buffer_rsrc_t rw = __builtin_amdgcn_make_buffer_rsrc(ws, 0, WS_END, 0x00020000);
  const u32 q = lane >> 4;

  if (__builtin_expect(wave < 4, 1)) {
    const u32 u = (u32)wave;
    const i32x4 sx = make_srd(x, (mt0 + nm) * 16u * 1152u), sw2 = make_srd(w2, 4608u * 1152u);
    if (__builtin_expect(u < 2, 1)) {
      __builtin_amdgcn_s_setprio(3);
      const i32x4 sw1 = make_srd(w1, 576u * 9216u);
      const u32 par = u;
      const u32 vx = (lane >> 3) * 1152u + ((lane & 7) ^ (4 * par + (lane >> 4))) * 16u;
      const u32 vw = (lane >> 4) * 9216u + (((lane & 15) >> 1) ^ ((lane >> 4) | (par << 2))) * 32u + (lane & 1) * 16u;
      const u32 oxb = (mt0 * 16 + 8 * par) * 1152u, owb = par * 8u * 9216u + (s * 9 + g) * 256u;
      const u32 mxb = par * 1024u, mwb = 10240u + par * 2048u;
      u32 kp = rot, slot = 0;
      auto st1 = [&]() {
        issue1(sx, sw1, vx, vw, oxb + kp * 128u, owb + kp * (64u * 9216u), mxb + slot, mwb + slot);
        kp = kp == 8u ? 0u : kp + 1u; slot = slot == 5u * SLOT ? 0u : slot + SLOT;
      };
      STAMP(5);
      st1();
      STAMP(6);
      st1();
      STAMP(1);
      const u32 v2 = (lane >> 3) * 1152u + (((lane & 7) >> 1) ^ ((lane >> 4) & 3)) * 32u + (lane & 1) * 16u;
      const u32 o2b = (s * 1152 + 8 * par) * 1152u + g * 128u;
      u32 gp = g, area = (u32)w2area(0);
      auto st2 = [&]() {
        issue2(sw2, v2, o2b + gp * (128u * 1152u), mxb + area);
        gp = gp == 8u ? 0u : gp + 1u; area = area == (u32)w2area(4) ? 0u : area + 16384u;
      };
#pragma unroll 1
      for (int j = 0; j < 7; ++j) {
        WAITV_BAR(13); st1();
      }
      WAITV_BAR(13); st2(); st2();
      STAMP(2);
      WAITV_BAR(16); st2();
#pragma unroll 1
      for (u32 i = 0; i < 6; ++i) {
        WAITV(16); LDSW(F_W2 + par * 4, i + 1);
        if (i == 3) wait4(L + LDS_FLAGS + F_P1, 1u);
        st2();
      }
      WAITV(16); LDSW(F_W2 + par * 4, 7u);
      WAITV(8); LDSW(F_W2 + par * 4, 8u);
      WAITV(0); LDSW(F_W2 + par * 4, 9u);
      STAMP(3);
      __builtin_amdgcn_s_setprio(0);
    } else {
      const i32x4 sws = make_srd(ws, WS_END);
      if (u == 2u) {
        if (PF_X) {
          const u32 l = g * 63u + lane, st = (l * 205u) >> 14, row = l - st * 80u;
          u32 kc = rot + 2u + st; kc = kc >= 9u ? kc - 9u : kc;
          touch(sx, (lane < 63u && st < 7u) ? row * 1152u + kc * 128u : 0x80000000u, mt0 * 16u * 1152u);
        }
      } else {
        if (PF_CODE && (b >> 3) < 3u) {
          u64 pc; u32 off, len;
          asm volatile("s_getpc_b64 %0\n9:\n\ts_mov_b32 %1, 9b-ffn_kernel\n\ts_mov_b32 %2, .Lffn_tail-ffn_kernel" : "=s"(pc), "=s"(off), "=s"(len));
          touch(make_srd((const void*)(pc - off), len), lane * 128u, (b >> 3) * 8192u);
        }
        if (PF_TLB) {
          const u32 a0 = WS_H + (mb * 4 + s) * (5u * 36864u), a1 = WS_P + (mb * 9 + g) * 16u * 5120u;
          touch1(sws, lane == 0u ? a0 : lane == 1u ? a0 + 184316u : lane == 2u ? a1 : lane == 3u ? a1 + 81916u : 0x80000000u, 0u);
          touch(make_srd(out, 589824u), lane == 0u ? mt0 * 16u * 1152u : 0x80000000u, 0u);
        }
      }
      const u32 Ev = __builtin_amdgcn_raw_buffer_load_b32(rw, 0, mb * 256, 16);
      if (lane < 32u) LDSW(lane * 4, 0u);
      BAR_L();
      if (PGW) {
        u64 pc; u32 o0, o1;
        asm volatile("s_getpc_b64 %0\n8:\n\ts_mov_b32 %1, .Lpg0-8b\n\ts_mov_b32 %2, .Lpg1-8b" : "=s"(pc), "=s"(o0), "=s"(o1));
        PGCALL;
      }
      if (PF_W2) {
        const u32 part = mb == 3u ? 3u : (mb & 3u);
        const u32 o = (s * 1152u + part * 288u + (u - 2u) * 144u) * 1152u + g * 128u;
        touch(sw2, lane * 1152u, o); touch(sw2, lane * 1152u, o + 64u * 1152u);
        touch(sw2, lane < 16u ? lane * 1152u : 0x80000000u, o + 128u * 1152u);
      }
#pragma unroll 1
      for (int j = 1; j < 7; ++j) BAR();
      LDSW(F_T, ~Ev);
      BAR_L();
      BAR();
      if (PF_H) {
        wait4(L + LDS_FLAGS + F_ACK, u - 1u);
        const u32 hg = WS_H + (mb * 4 + s) * (5u * 36864u) + g * 4096u + (u - 2u) * (2u * 36864u);
        if (PF_H == 1) touch1(sws, 36864u + (lane >> 5) * 36864u + (lane & 31u) * 128u, hg);
        if (PF_H == 2) { touch1(sws, 36864u + lane * 64u, hg); touch1(sws, 2u * 36864u + lane * 64u, hg); }
        if (PF_H == 3) {
#pragma unroll
          for (u32 k = 0; k < 4; ++k) touch1(sws, 36864u + (k >> 1) * 36864u + (k & 1u) * 2048u + lane * 32u, hg);
        }
      }
    }
    asm volatile("s_branch 2f\n\t.p2align 12\n2:" ::: "memory");
    const u32 T = __builtin_amdgcn_readfirstlane(*(volatile AS3 u32*)(L + LDS_FLAGS + F_T));
    const u32 nib = T & 15u;
    u32x2 bb = *(const u32x2*)(b2 + g * 64 + s * 16 + q * 4);
    asm volatile("" : "+v"(bb));
    auto lsum = [&](u32 mt, u32 t) {
      const lptr r = L + (mt < 3u ? (u32)w2area(2) + mt * 16384u : (mt - 3u) * 16384u) + t * 1024 + lane * 16;
      const f32x4 a0 = *(AS3 f32x4*)r, a1 = *(AS3 f32x4*)(r + 4096), a2 = *(AS3 f32x4*)(r + 8192), a3 = *(AS3 f32x4*)(r + 12288);
      return (a0 + a1) + (a2 + a3);
    };
    if (u != s) {
      const u32 pout = WS_P + (((mb * 9 + g) * 4 + u) * 4 + s) * 5120u;
#pragma unroll 1
      for (u32 mt = 0; mt < nm; ++mt) {
        wait1(L + LDS_FLAGS + F_PART + mt * 4, 0x01010101u);
        const u32x4 w = __builtin_bit_cast(u32x4, lsum(mt, u));
        __builtin_amdgcn_raw_buffer_store_b128((w & ~15u) | nib, rw, lane * 16, pout + mt * 1024, 16);
        if (mt == 0) STAMP(4);
      }
    }
    STAMP(8);
    const u32 pin = WS_P + ((mb * 9 + g) * 4 + s) * 4 * 5120u;
    u32 so[3];
#pragma unroll
    for (u32 k = 0; k < 3; ++k) so[k] = pin + ((s + 1u + k) & 3u) * 5120u;
    const __amdgpu_buffer_rsrc_t ro = __builtin_amdgcn_make_buffer_rsrc(out, 0, 589824u, 0x00020000);
    const u32 oo = (mt0 * 16u + (lane & 15u)) * 1152u + (g * 64u + s * 16u + q * 4u) * 2u;
    u32 mt = (u - s) & 3u;
    if (mt == 0u || mt + 1u < nm) {
#pragma unroll 1
      for (;;) {
        wait1(L + LDS_FLAGS + F_PART + mt * 4, 0x01010101u);
        const u32 vi = lane * 16 + mt * 1024;
        u32x4 p[3], r[3];
        auto ld = [&](u32x4* d) {
#pragma unroll
          for (int k = 0; k < 3; ++k) d[k] = __builtin_amdgcn_raw_buffer_load_b128(rw, vi, so[k], 16);
          asm volatile("" ::: "memory");
        };
        auto ok = [&](const u32x4* d) {
          u32 bad = 0;
#pragma unroll
          for (int k = 0; k < 3; ++k) bad |= (d[k].x ^ nib) | (d[k].y ^ nib) | (d[k].z ^ nib) | (d[k].w ^ nib);
          return !any_lane((bad & 15u) != 0u);
        };
        ld(p);
        f32x4 v = lsum(mt, s);
        asm volatile("" : "+v"(v));
        __builtin_amdgcn_s_sleep(POLL_GAP);
#pragma unroll 1
        for (int it = 0; it < SPIN_MAX; ++it) {
          ld(r);
          if (ok(p)) break;
          ld(p);
          if (ok(r)) { p[0] = r[0]; p[1] = r[1]; p[2] = r[2]; break; }
        }
        STAMP(11);
#pragma unroll
        for (int k = 0; k < 3; ++k) v += __builtin_bit_cast(f32x4, p[k] & ~15u);
        const u32 p0 = pk(v.x, v.y), p1 = pk(v.z, v.w);
        u32x2 o;
        o.x = pk(lo(p0) + lo(bb.x), hi(p0) + hi(bb.x));
        o.y = pk(lo(p1) + lo(bb.y), hi(p1) + hi(bb.y));
        __builtin_amdgcn_raw_buffer_store_b64(o, ro, oo, mt * (16 * 1152), OUT_AUX);
        if (mt != 0u) break;
        STAMP(9);
        mt = nm - 1u;
      }
      if (u == 0u && s == 0u && g == 0u && lane == 0u) __builtin_amdgcn_raw_buffer_store_b32(T, rw, 0, mb * 256, 16);
    }
    STAMP(10);
    FLUSH();
    return;
  }

  const u32 w = (u32)wave - 4u;
  if (CPRIO) __builtin_amdgcn_s_setprio(CPRIO);
  const u32 a = (lane >> 2) & 3, p = lane & 3, m = lane & 15;
  const u32x2 bA = *(const u32x2*)(b1 + (s * 9 + g) * 128 + w * 32 + q * 4);
  const u32x2 bB = *(const u32x2*)(b1 + (s * 9 + g) * 128 + w * 32 + 16 + q * 4);
  const u32 Ev = __builtin_amdgcn_raw_buffer_load_b32(rw, 0, mb * 256, 16);
  f32x4 acc[2][5];
#pragma unroll
  for (int t = 0; t < 2; ++t)
#pragma unroll
    for (int mt = 0; mt < 5; ++mt) acc[t][mt] = (f32x4){0.f, 0.f, 0.f, 0.f};
  {
    const u32 xa0 = m * 128u + (q ^ (m >> 1)) * 16u;
    const u32 wa0 = 10240u + (8 * q + a) * 256u + ((2 * w) ^ (a | ((q & 1) << 2))) * 32u + p * 8u;
    bf16x8 A[2][2], B[2][5];
    auto rd = [&](int ks, u32 slot) {
#pragma unroll
      for (int t = 0; t < 2; ++t) {
        const lptr pw = L + (slot + (wa0 ^ (t * 32u))) + ks * 8192;
        A[ks][t] = tr2(pw, pw + 1024);
      }
#pragma unroll
      for (int mt = 0; mt < 5; ++mt) B[ks][mt] = *(AS3 bf16x8*)(L + (slot + (xa0 ^ (ks * 64u))) + mt * 2048);
    };
    auto mm = [&](int ks) {
#pragma unroll
      for (int mt = 0; mt < 5; ++mt)
#pragma unroll
        for (int t = 0; t < 2; ++t) acc[t][mt] = __builtin_amdgcn_mfma_f32_16x16x32_bf16(A[ks][t], B[ks][mt], acc[t][mt], 0, 0, 0);
    };
    auto mix = [&]() {
#pragma unroll
      for (int k = 0; k < 2; ++k) { __builtin_amdgcn_sched_group_barrier(0x008, 1, 0); __builtin_amdgcn_sched_group_barrier(0x100, 2, 0); }
#pragma unroll
      for (int k = 0; k < 5; ++k) { __builtin_amdgcn_sched_group_barrier(0x008, 1, 0); __builtin_amdgcn_sched_group_barrier(0x100, 1, 0); }
      __builtin_amdgcn_sched_group_barrier(0x008, 3, 0);
    };
    u32 slot = 0;
    BAR();
    rd(0, 0u);
#pragma unroll 1
    for (u32 j = 0; j < 9; ++j) {
      __builtin_amdgcn_sched_barrier(0);
      rd(1, slot); mm(0); mix();
      __builtin_amdgcn_sched_barrier(0);
      if (j == 3) STAMP(15);
      asm volatile("s_cmp_eq_u32 %0, 8\n\ts_cbranch_scc1 1f\n\ts_barrier\n\t1:" :: "s"(j) : "memory", "scc");
      if (j == 3) STAMP(16);
      slot = slot == 5u * SLOT ? 0u : slot + SLOT;
      __builtin_amdgcn_sched_barrier(0);
      rd(0, slot); mm(1); mix();
      __builtin_amdgcn_sched_barrier(0);
      if (j == 3) STAMP(17);
      if (j == 2) STAMP(18);
    }
    __builtin_amdgcn_sched_barrier(0);
  }
  asm volatile("s_branch 2f\n\t" PAGE(".Lpg0") "2:\n\ts_waitcnt lgkmcnt(0)" ::: "memory");
  LDSW(F_P1 + w * 4, 1u);
  STAMP(1);
  const u32 T = ~__builtin_amdgcn_readfirstlane(Ev);
  const i32x4 sws = make_srd(ws, WS_END);
  const f32x2 bv[4] = {{lo(bA.x), hi(bA.x)}, {lo(bA.y), hi(bA.y)}, {lo(bB.x), hi(bB.x)}, {lo(bB.y), hi(bB.y)}};
  u32 vo = lane * 16u;
  const u32 S = ((T >> 15) & 1u) * 0x80008000u;
  const u32 hb = WS_H + (mb * 4 + s) * (5u * 36864u) + w * 1024u;
  u32 so[9];
#pragma unroll
  for (u32 i = 0; i < 9; ++i) so[i] = hb + (g + i >= 9 ? g + i - 9 : g + i) * 4096u;
  bf16x8 W[9][4];
  u32x4 H[9];
  u32 have = 0;
  const u32 wb = (32 * w + 4 * q + a) * 128u + ((2 * q + (a >> 1)) & 3) * 32u + p * 8u;
  const u32 lw = w * 4096u + lane * 16u;
  auto rdw = [&](int i) {
    wait2(L + LDS_FLAGS + F_W2, (u32)i + 1u, have);
    const int base = i < 4 ? w2area(0) : i == 4 ? w2area(4) : 0;
#pragma unroll
    for (int t = 0; t < 4; ++t) {
      u32 pa = (wb ^ (t * 32u)) + base;
      asm("" : "+v"(pa));
      const lptr pw = L + pa + (w2area(i) - base);
      W[i][t] = tr2(pw, pw + 2048);
    }
  };
  u32x4 hp;
#pragma unroll
  for (int i = 0; i < 5; ++i) {
    u32x4 h;
    h.x = act2(acc[0][i].x, acc[0][i].y, bv[0]); h.y = act2(acc[0][i].z, acc[0][i].w, bv[1]);
    h.z = act2(acc[1][i].x, acc[1][i].y, bv[2]); h.w = act2(acc[1][i].z, acc[1][i].w, bv[3]);
    asm volatile("buffer_store_dwordx4 %0, %1, %2, %3 offen sc1\n\ts_nop 1" :: "v"(h | S), "v"((i < 4 || nm == 5u) ? vo + i * 36864u : 0x80000000u), "s"(sws), "s"(so[0]) : "memory");
    if (i == 0) { hp = h; for (int k = 0; k < WQ0; ++k) rdw(k); }
    if (i == 1) { for (int k = WQ0; k < WQ1; ++k) rdw(k); *(AS3 u32x4*)(L + w2area(0) + lw) = hp; }
    if (i == 3) { for (int k = WQ1; k < WQ3; ++k) rdw(k); }
    if (i >= 1) *(AS3 u32x4*)(L + (i < 4 ? w2area(0) + i * 1024 : w2area(1)) + lw) = h;
    __builtin_amdgcn_sched_barrier(0);
  }
  STAMP(2);
  HLOAD(1, 224, 227); HLOAD(2, 228, 231); HLOAD(3, 232, 235); HLOAD(4, 236, 239);
  HLOAD(5, 240, 243); HLOAD(6, 244, 247); HLOAD(7, 248, 251); HLOAD(8, 252, 255);
  if (PF_H) { WAITV(10); LDSW(F_ACK + w * 4, 1u); }
#pragma unroll
  for (int k = WQ3; k < 9; ++k) rdw(k);
  STAMP(3);
  u32 cnt = SPIN_MAX, t, first = 1, mt = 0;
  u32 ra = (u32)w2area(2) + lw, oh = (u32)w2area(0) + lw, fa = LDS_FLAGS + F_PART + w;
  u32 vn = vo + 36864u;
#define GC(a, b, c, d) "v_bitop3_b32 %[t1], v" #a ", v" #b ", %[S] bitop3:0x7e\n\tv_bitop3_b32 %[t2], v" #c ", v" #d ", %[S] bitop3:0x7e\n\tv_or3_b32 %[t], %[t], %[t1], %[t2]\n\t"
#define GL(a, b, k) "buffer_load_dwordx4 v[" #a ":" #b "], %[vr], %[rw], %[s" #k "] offen sc1\n\t"
#pragma unroll 1
  for (;;) {
    u32 t1, t2;
    asm volatile("s_cmp_eq_u32 %[first], 0\n\ts_cbranch_scc1 3f\n\t"
                 "1:\n\ts_waitcnt vmcnt(0)\n\tv_mov_b32 %[t], 0\n\t"
                 GC(224, 225, 226, 227) GC(228, 229, 230, 231) GC(232, 233, 234, 235) GC(236, 237, 238, 239)
                 GC(240, 241, 242, 243) GC(244, 245, 246, 247) GC(248, 249, 250, 251) GC(252, 253, 254, 255)
                 "v_and_b32 %[t], %[M], %[t]\n\tv_cmp_ne_u32 vcc, 0, %[t]\n\ts_cbranch_vccz 2f\n\t"
                 "s_cmp_eq_u32 %[cnt], 0\n\ts_cbranch_scc1 2f\n\t3:\n\ts_sub_u32 %[cnt], %[cnt], 1\n\t"
                 GL(224, 227, 1) GL(228, 231, 2) GL(232, 235, 3) GL(236, 239, 4) GL(240, 243, 5) GL(244, 247, 6) GL(248, 251, 7) GL(252, 255, 8)
                 "s_branch 1b\n\t" PAGE(".Lpg1") "2:\n\t" X2(224, 225) X2(226, 227) "s_nop 1"
                 : [t] "=&v"(t), [t1] "=&v"(t1), [t2] "=&v"(t2), [cnt] "+s"(cnt), "+{v[224:227]}"(H[1]), "+{v[228:231]}"(H[2]),
                   "+{v[232:235]}"(H[3]), "+{v[236:239]}"(H[4]), "+{v[240:243]}"(H[5]), "+{v[244:247]}"(H[6]), "+{v[248:251]}"(H[7]), "+{v[252:255]}"(H[8])
                 : [S] "s"(S), [M] "s"(0x80008000u), [vr] "v"(vo), [rw] "s"(sws), [s1] "s"(so[1]), [s2] "s"(so[2]), [s3] "s"(so[3]), [s4] "s"(so[4]),
                   [s5] "s"(so[5]), [s6] "s"(so[6]), [s7] "s"(so[7]), [s8] "s"(so[8]), [first] "s"(first)
                 : "memory", "vcc", "scc");
    if (PF_H) LDSW(F_ACK + w * 4, 2u);
    if (first) STAMP(5);
    first = 0;
    f32x4 o[4];
    STEP_X(1, 224, 227, 228, 229, 230, 231, "0", "0", "0", "0", "=&v");
    bool done = false;
#pragma unroll 1
    for (;;) {
      asm volatile("ds_read_b128 %0, %1" : "=v"(H[0]) : "v"(oh) : "memory");
      STEP_X(2, 228, 231, 232, 233, 234, 235, ACC);
      STEP_X(3, 232, 235, 236, 237, 238, 239, ACC);
      STEP_X(4, 236, 239, 240, 241, 242, 243, ACC);
      STEP_X(5, 240, 243, 244, 245, 246, 247, ACC);
      STEP_X(6, 244, 247, 248, 249, 250, 251, ACC);
      STEP_X(7, 248, 251, 252, 253, 254, 255, ACC);
      u64 bad;
      asm volatile(MF("%[o0]", "%[w0]", "v[252:255]", "%[o0]") "v_and_b32 %[t], %[M], %[t]\n\ts_waitcnt lgkmcnt(0)\n\t"
                   MF("%[o0]", "%[u0]", "%[h]", "%[o0]") "v_cmp_ne_u32 %[bad], 0, %[t]\n\t" WV6
                   MF("%[o1]", "%[w1]", "v[252:255]", "%[o1]") X2(224, 225)
                   MF("%[o1]", "%[u1]", "%[h]", "%[o1]") X2(226, 227)
                   MF("%[o2]", "%[w2]", "v[252:255]", "%[o2]") DW("%[o0]", 0)
                   MF("%[o2]", "%[u2]", "%[h]", "%[o2]") "v_or_b32 %[t], v224, v225\n\tv_or3_b32 %[t], %[t], v226, v227\n\t"
                   MF("%[o3]", "%[w3]", "v[252:255]", "%[o3]") RF(252, 255) DW("%[o1]", 1024)
                   MF("%[o3]", "%[u3]", "%[h]", "%[o3]")
                   : [o0] "+v"(o[0]), [o1] "+v"(o[1]), [o2] "+v"(o[2]), [o3] "+v"(o[3]), [t] "+v"(t), [bad] "=&s"(bad),
                     "+{v[252:255]}"(H[8]), "+{v[224:227]}"(H[1])
                   : WOPS(8), [u0] "v"(W[0][0]), [u1] "v"(W[0][1]), [u2] "v"(W[0][2]), [u3] "v"(W[0][3]), [h] "v"(H[0]),
                     [S] "s"(S), [M] "s"(0x80008000u), [vn] "v"(vn), [rw] "s"(sws), [sc] "s"(so[8]), [ra] "v"(ra)
                   : "memory");
      if (__builtin_expect(bad != 0ull && cnt != 0u, 0)) break;
      ++mt;
      vo = vn;
      vn = mt + 1u < nm ? vn + 36864u : 0x80000000u;
      asm volatile(MF("%[o0]", "%[w0]", "v[224:227]", "0") DW("%[o2]", 2048) WV6 X2(228, 229)
                   MF("%[o1]", "%[w1]", "v[224:227]", "0") X2(230, 231)
                   MF("%[o2]", "%[w2]", "v[224:227]", "0") DW("%[o3]", 3072) "ds_write_b8 %[fa], %[one]\n\t" O2(228, 229, 230, 231)
                   MF("%[o3]", "%[w3]", "v[224:227]", "0") RF(224, 227)
                   : [o0] "+v"(o[0]), [o1] "+v"(o[1]), [o2] "+v"(o[2]), [o3] "+v"(o[3]), [t] "+v"(t),
                     "+{v[224:227]}"(H[1]), "+{v[228:231]}"(H[2])
                   : WOPS(1), [S] "s"(S), [vn] "v"(vn), [rw] "s"(sws), [sc] "s"(so[1]), [ra] "v"(ra), [fa] "v"(fa), [one] "v"(1u)
                   : "memory");
      if (mt == 1u) STAMP(4);
      if (mt == nm) { done = true; break; }
      ra += mt == 3u ? 0u - (u32)w2area(4) : 16384u;
      oh += mt == 4u ? 16384u - 3072u : 1024u;
      fa += 4u;
    }
    if (done) break;
  }
  STAMP(8);
  asm volatile(".Lffn_tail:");
  FLUSH();
}

extern "C" void kernel_launch(void* const* d_in, const int* in_sizes, int n_in, void* d_out, int out_size, void* d_ws,
                              size_t ws_size, hipStream_t stream) {
  (void)in_sizes; (void)n_in; (void)out_size; (void)ws_size;
  (void)hipFuncSetAttribute((const void*)ffn_kernel, hipFuncAttributeMaxDynamicSharedMemorySize, LDS_BYTES);
  ffn_kernel<<<256, 512, LDS_BYTES, stream>>>((const u16*)d_in[0], (const u16*)d_in[1], (const u16*)d_in[2],
                                              (const u16*)d_in[3], (const u16*)d_in[4], (u16*)d_out, (char*)d_ws);
}
```

```cpp
#include <hip/hip_runtime.h>

typedef unsigned short u16;
typedef unsigned int u32;
typedef unsigned long long u64;
typedef __bf16 bf16x8 __attribute__((ext_vector_type(8)));
typedef __bf16 bf16x2 __attribute__((ext_vector_type(2)));
typedef short s16x4 __attribute__((ext_vector_type(4)));
typedef short s16x8 __attribute__((ext_vector_type(8)));
typedef float f32x4 __attribute__((ext_vector_type(4)));
typedef float f32x2 __attribute__((ext_vector_type(2)));
typedef u32 u32x4 __attribute__((ext_vector_type(4)));
typedef u32 u32x2 __attribute__((ext_vector_type(2)));
typedef int i32x4 __attribute__((ext_vector_type(4)));
#define AS3 __attribute__((address_space(3)))
#define AS4 __attribute__((address_space(4)))
typedef AS3 char* lptr;

constexpr int SLOT = 26 * 1024;
constexpr int LDS_FLAGS = 158 * 1024;
constexpr int LDS_BYTES = LDS_FLAGS + 256;
constexpr int F_PART = 0;
constexpr int F_W2 = 80;
constexpr int F_P1 = 96;
constexpr int F_ACK = 112;
constexpr int F_T = 128;
__host__ __device__ constexpr int w2area(int i) { return i < 5 ? (78 + 16 * i) * 1024 : (i - 5) * 16384; }
constexpr u32 WS_H = 4096;
constexpr u32 WS_P = WS_H + 7u * 4 * 5 * 36 * 1024;
constexpr u32 WS_END = WS_P + 7u * 9 * 16 * 5 * 1024;
constexpr int SPIN_MAX = 1 << 16;
#define OUT_AUX 17
#define PF_X 1
#define PF_W2 1
#define PF_CODE 1
#define PF_H 1
#define PF_TLB 1
#define POLL_GAP 4
#define W1SLEEP 1
#define CPRIO 2
#define WQ0 2
#define WQ1 2
#define WQ3 3
#define PGW 1
#define PAGE(l) ".p2align 12\n" l ":\n\ts_setpc_b64 vcc\n"
#define PGCALL asm volatile("s_swappc_b64 vcc, %0" :: "s"(pc + (u64)(long long)(int)(u == 2u ? o0 : o1)) : "vcc", "memory")
#define DEPTH 2

__device__ __forceinline__ i32x4 make_srd(const void* p, u32 bytes) {
  u64 a = (u64)p;
  i32x4 r; r.x = (int)(u32)a; r.y = (int)(u32)(a >> 32) & 0xffff; r.z = (int)bytes; r.w = 0x00020000;
  return r;
}
#define BAR() asm volatile("s_barrier" ::: "memory")
#define BAR_L() asm volatile("s_waitcnt lgkmcnt(0)\n\ts_barrier" ::: "memory")
#define WAITV_BAR(n) asm volatile("s_waitcnt vmcnt(" #n ")\n\ts_barrier" ::: "memory")

#define DMA(v, s, o) "buffer_load_dwordx4 " v ", " s ", " o " offen lds\n\t"
#define STEP(o, ms, os) "s_add_u32 m0, m0, " #ms "\n\ts_add_u32 " o ", " o ", " #os "\n\t"
__device__ __forceinline__ void issue1(i32x4 sx, i32x4 sw, u32 vx, u32 vw, u32 ox, u32 ow, u32 mx, u32 mw) {
  asm volatile(
      "s_nop 4\n\t"
      "s_mov_b32 m0, %7\n\ts_nop 0\n\t"
      DMA("%3", "%5", "%1") STEP("%1", 1024, 36864) DMA("%3", "%5", "%1") STEP("%1", 3072, 110592)
      DMA("%3", "%5", "%1") STEP("%1", 1024, 36864) DMA("%3", "%5", "%1") STEP("%1", 3072, 110592)
      DMA("%3", "%5", "%1") STEP("%1", 1024, 36864) DMA("%3", "%5", "%1") STEP("%1", 3072, 110592)
      DMA("%3", "%5", "%1") STEP("%1", 1024, 36864) DMA("%3", "%5", "%1")
      "s_mov_b32 m0, %6\n\ts_nop 0\n\t"
      DMA("%2", "%4", "%0") STEP("%0", 2048, 18432) DMA("%2", "%4", "%0") STEP("%0", 2048, 18432)
      DMA("%2", "%4", "%0") STEP("%0", 2048, 18432) DMA("%2", "%4", "%0") STEP("%0", 2048, 18432)
      DMA("%2", "%4", "%0")
      : "+s"(ox), "+s"(ow) : "v"(vx), "v"(vw), "s"(sx), "s"(sw), "s"(mx), "s"(mw) : "memory", "scc");
}
__device__ __forceinline__ void issue2(i32x4 sw, u32 v, u32 o, u32 m) {
  asm volatile(
      "s_nop 4\n\t"
      "s_mov_b32 m0, %3\n\ts_nop 0\n\t"
      DMA("%1", "%2", "%0") STEP("%0", 2048, 18432) DMA("%1", "%2", "%0") STEP("%0", 2048, 18432)
      DMA("%1", "%2", "%0") STEP("%0", 2048, 18432) DMA("%1", "%2", "%0") STEP("%0", 2048, 18432)
      DMA("%1", "%2", "%0") STEP("%0", 2048, 18432) DMA("%1", "%2", "%0") STEP("%0", 2048, 18432)
      DMA("%1", "%2", "%0") STEP("%0", 2048, 18432) DMA("%1", "%2", "%0")
      : "+s"(o) : "v"(v), "s"(sw), "s"(m) : "memory", "scc");
}
__device__ __forceinline__ bf16x8 tr2(lptr p0, lptr p1) {
  s16x4 a = __builtin_amdgcn_ds_read_tr16_b64_v4i16((AS3 s16x4*)p0);
  s16x4 b = __builtin_amdgcn_ds_read_tr16_b64_v4i16((AS3 s16x4*)p1);
  s16x8 c = __builtin_shufflevector(a, b, 0, 1, 2, 3, 4, 5, 6, 7);
  return __builtin_bit_cast(bf16x8, c);
}
__device__ __forceinline__ u32 pk(float a, float b) {
  bf16x2 v = {(__bf16)a, (__bf16)b};
  return __builtin_bit_cast(u32, v);
}
__device__ __forceinline__ float lo(u32 p) { return __uint_as_float(p << 16); }
__device__ __forceinline__ float hi(u32 p) { return __uint_as_float(p & 0xffff0000u); }
__device__ __forceinline__ float relu(float v) {
  float r; asm("v_max_f32 %0, 0, %1" : "=v"(r) : "v"(v)); return r;
}
__device__ __forceinline__ u32 act2(float a0, float a1, f32x2 bv) {
  u32 p = pk(a0, a1);
  asm("" : "+v"(p));
  f32x2 v = {lo(p), hi(p)};
  v += bv;
  p = pk(v.x, v.y);
  asm("" : "+v"(p));
  f32x2 r = {relu(lo(p)), relu(hi(p))};
  r *= r;
  return pk(r.x, r.y);
}
#define WV6 "s_waitcnt vmcnt(6)\n\t"
#define MF(o, wv, h, c) "v_mfma_f32_16x16x32_bf16 " o ", " wv ", " h ", " c "\n\t"
#define HR(a, b) "v[" #a ":" #b "]"
#define X2(a, b) "v_xor_b32 v" #a ", %[S], v" #a "\n\tv_xor_b32 v" #b ", %[S], v" #b "\n\t"
#define O2(a, b, c, d) "v_or3_b32 %[t], %[t], v" #a ", v" #b "\n\tv_or3_b32 %[t], %[t], v" #c ", v" #d "\n\t"
#define RF(c0, c3) "buffer_load_dwordx4 " HR(c0, c3) ", %[vn], %[rw], %[sc] offen sc1\n\t"
#define DW(o, off) "ds_write_b128 %[ra], " o " offset:" #off "\n\t"
#define WOPS(I) [w0] "v"(W[I][0]), [w1] "v"(W[I][1]), [w2] "v"(W[I][2]), [w3] "v"(W[I][3])
#define STEP_MID(I, c0, c3, n0, n1, n2, n3, C0, C1, C2, C3, OC) \
  asm volatile(MF("%[o0]", "%[w0]", HR(c0, c3), C0) WV6 X2(n0, n1) MF("%[o1]", "%[w1]", HR(c0, c3), C1) \
      X2(n2, n3) MF("%[o2]", "%[w2]", HR(c0, c3), C2) O2(n0, n1, n2, n3) MF("%[o3]", "%[w3]", HR(c0, c3), C3) RF(c0, c3) \
      : [o0] OC(o[0]), [o1] OC(o[1]), [o2] OC(o[2]), [o3] OC(o[3]), [t] "+v"(t), \
        "+{" HR(c0, c3) "}"(H[I]), "+{" HR(n0, n3) "}"(H[I + 1]) \
      : WOPS(I), [S] "s"(S), [vn] "v"(vn), [rw] "s"(sws), [sc] "s"(so[I]) : "memory")
#define ACC "%[o0]", "%[o1]", "%[o2]", "%[o3]", "+v"
#define STEP_X(...) STEP_MID(__VA_ARGS__)
#define HLOAD(I, c0, c3) \
  asm volatile("buffer_load_dwordx4 " HR(c0, c3) ", %1, %2, %3 offen sc1" : "={" HR(c0, c3) "}"(H[I]) : "v"(vo), "s"(sws), "s"(so[I]) : "memory")
__device__ __forceinline__ bool any_lane(bool c) { return __builtin_amdgcn_ballot_w64(c) != 0; }

#define LDSW(off, v) (*(volatile AS3 u32*)(L + LDS_FLAGS + (off)) = (v))
__device__ __forceinline__ void touch(i32x4 srd, u32 voff, u32 soff) {
  asm volatile("buffer_load_dword v255, %0, %1, %2 offen" :: "v"(voff), "s"(srd), "s"(soff) : "memory", "v255");
}
__device__ __forceinline__ void touch1(i32x4 srd, u32 voff, u32 soff) {
  asm volatile("buffer_load_dword v255, %0, %1, %2 offen sc1" :: "v"(voff), "s"(srd), "s"(soff) : "memory", "v255");
}
__device__ __forceinline__ u32 min4(u32x4 f) { return min(min(f.x, f.y), min(f.z, f.w)); }
__device__ __forceinline__ void wait4(lptr p, u32 need) {
#pragma unroll 1
  for (int it = 0; it < (SPIN_MAX << 6); ++it) {
    const u32x4 f = *(volatile AS3 u32x4*)p;
    if (__builtin_amdgcn_readfirstlane(min4(f)) >= need) break;
    __builtin_amdgcn_s_sleep(1);
  }
  asm volatile("" ::: "memory");
}
__device__ __forceinline__ void wait2(lptr p, u32 need, u32& have) {
  if (__builtin_expect(have < need, 0)) {
#pragma unroll 1
    for (int it = 0; have < need && it < (SPIN_MAX << 6); ++it) {
      const u32x2 f = *(volatile AS3 u32x2*)p;
      have = __builtin_amdgcn_readfirstlane(min(f.x, f.y));
    }
  }
  asm volatile("" ::: "memory");
}
__device__ __forceinline__ void wait1(lptr p, u32 need) {
#pragma unroll 1
  for (int it = 0; it < (SPIN_MAX << 6); ++it) {
    if (__builtin_amdgcn_readfirstlane(*(volatile AS3 u32*)p) >= need) break;
    __builtin_amdgcn_s_sleep(W1SLEEP);
  }
  asm volatile("" ::: "memory");
}
#define WAITV(n) asm volatile("s_waitcnt vmcnt(" #n ")" ::: "memory")

extern "C" __global__ void __launch_bounds__(512, 2)
ffn_kernel(const u16* __restrict__ x, const u16* __restrict__ w1, const u16* __restrict__ b1,
           const u16* __restrict__ w2, const u16* __restrict__ b2, u16* __restrict__ out, char* ws) {
  extern __shared__ __attribute__((aligned(16))) char lds_[];
  const lptr L = (lptr)lds_;
  const u32 b = blockIdx.x;
  const u32 s = (b & 7) >> 1, idx = (b & 1) * 32 + (b >> 3);
  if (idx >= 63) return;
  const u32 mb = (idx * 57) >> 9, g = idx - mb * 9;
  const u32 mtp = (u32)(0x201C18130F0A0500ull >> (mb * 8));
  const u32 mt0 = mtp & 255u, nm = ((mtp >> 8) & 255u) - mt0;
  const u32 rot = (0x4207420u >> (mb * 4)) & 15u;
  const int wave = __builtin_amdgcn_readfirstlane(threadIdx.x >> 6);
  const u32 lane = threadIdx.x & 63;
  asm volatile("" :: "s"(x), "s"(w1), "s"(b1), "s"(w2), "s"(b2), "s"(out), "s"(ws));
  const __amdgpu_buffer_rsrc_t rw = __builtin_amdgcn_make_buffer_rsrc(ws, 0, WS_END, 0x00020000);
  const u32 q = lane >> 4;

  if (__builtin_expect(wave < 4, 1)) {
    const u32 u = (u32)wave;
    const i32x4 sx = make_srd(x, (mt0 + nm) * 16u * 1152u), sw2 = make_srd(w2, 4608u * 1152u);
    if (__builtin_expect(u < 2, 1)) {
      __builtin_amdgcn_s_setprio(3);
      const i32x4 sw1 = make_srd(w1, 576u * 9216u);
      const u32 par = u;
      const u32 vx = (lane >> 3) * 1152u + ((lane & 7) ^ (4 * par + (lane >> 4))) * 16u;
      const u32 vw = (lane >> 4) * 9216u + (((lane & 15) >> 1) ^ ((lane >> 4) | (par << 2))) * 32u + (lane & 1) * 16u;
      const u32 oxb = (mt0 * 16 + 8 * par) * 1152u, owb = par * 8u * 9216u + (s * 9 + g) * 256u;
      const u32 mxb = par * 1024u, mwb = 10240u + par * 2048u;
      u32 kp = rot, slot = 0;
      auto st1 = [&]() {
        issue1(sx, sw1, vx, vw, oxb + kp * 128u, owb + kp * (64u * 9216u), mxb + slot, mwb + slot);
        kp = kp == 8u ? 0u : kp + 1u; slot = slot == 5u * SLOT ? 0u : slot + SLOT;
      };
      st1();
      st1();
      const u32 v2 = (lane >> 3) * 1152u + (((lane & 7) >> 1) ^ ((lane >> 4) & 3)) * 32u + (lane & 1) * 16u;
      const u32 o2b = (s * 1152 + 8 * par) * 1152u + g * 128u;
      u32 gp = g, area = (u32)w2area(0);
      auto st2 = [&]() {
        issue2(sw2, v2, o2b + gp * (128u * 1152u), mxb + area);
        gp = gp == 8u ? 0u : gp + 1u; area = area == (u32)w2area(4) ? 0u : area + 16384u;
      };
#pragma unroll 1
      for (int j = 0; j < 7; ++j) {
        WAITV_BAR(13); st1();
      }
      WAITV_BAR(13); st2(); st2();
      WAITV_BAR(16); st2();
#pragma unroll 1
      for (u32 i = 0; i < 6; ++i) {
        WAITV(16); LDSW(F_W2 + par * 4, i + 1);
        if (i == 3) wait4(L + LDS_FLAGS + F_P1, 1u);
        st2();
      }
      WAITV(16); LDSW(F_W2 + par * 4, 7u);
      WAITV(8); LDSW(F_W2 + par * 4, 8u);
      WAITV(0); LDSW(F_W2 + par * 4, 9u);
      __builtin_amdgcn_s_setprio(0);
    } else {
      const i32x4 sws = make_srd(ws, WS_END);
      if (u == 2u) {
        if (PF_X) {
          const u32 l = g * 63u + lane, st = (l * 205u) >> 14, row = l - st * 80u;
          u32 kc = rot + 2u + st; kc = kc >= 9u ? kc - 9u : kc;
          touch(sx, (lane < 63u && st < 7u) ? row * 1152u + kc * 128u : 0x80000000u, mt0 * 16u * 1152u);
        }
      } else {
        if (PF_CODE && (b >> 3) < 3u) {
          u64 pc; u32 off, len;
          asm volatile("s_getpc_b64 %0\n9:\n\ts_mov_b32 %1, 9b-ffn_kernel\n\ts_mov_b32 %2, .Lffn_tail-ffn_kernel" : "=s"(pc), "=s"(off), "=s"(len));
          touch(make_srd((const void*)(pc - off), len), lane * 128u, (b >> 3) * 8192u);
        }
        if (PF_TLB) {
          const u32 a0 = WS_H + (mb * 4 + s) * (5u * 36864u), a1 = WS_P + (mb * 9 + g) * 16u * 5120u;
          touch1(sws, lane == 0u ? a0 : lane == 1u ? a0 + 184316u : lane == 2u ? a1 : lane == 3u ? a1 + 81916u : 0x80000000u, 0u);
          touch(make_srd(out, 589824u), lane == 0u ? mt0 * 16u * 1152u : 0x80000000u, 0u);
        }
      }
      const u32 Ev = __builtin_amdgcn_raw_buffer_load_b32(rw, 0, mb * 256, 16);
      if (lane < 32u) LDSW(lane * 4, 0u);
      BAR_L();
      if (PGW) {
        u64 pc; u32 o0, o1;
        asm volatile("s_getpc_b64 %0\n8:\n\ts_mov_b32 %1, .Lpg0-8b\n\ts_mov_b32 %2, .Lpg1-8b" : "=s"(pc), "=s"(o0), "=s"(o1));
        PGCALL;
      }
      if (PF_W2) {
        const u32 part = mb == 3u ? 3u : (mb & 3u);
        const u32 o = (s * 1152u + part * 288u + (u - 2u) * 144u) * 1152u + g * 128u;
        touch(sw2, lane * 1152u, o); touch(sw2, lane * 1152u, o + 64u * 1152u);
        touch(sw2, lane < 16u ? lane * 1152u : 0x80000000u, o + 128u * 1152u);
      }
#pragma unroll 1
      for (int j = 1; j < 7; ++j) BAR();
      LDSW(F_T, ~Ev);
      BAR_L();
      BAR();
      if (PF_H) {
        wait4(L + LDS_FLAGS + F_ACK, u - 1u);
        const u32 hg = WS_H + (mb * 4 + s) * (5u * 36864u) + g * 4096u + (u - 2u) * (2u * 36864u);
        if (PF_H == 1) touch1(sws, 36864u + (lane >> 5) * 36864u + (lane & 31u) * 128u, hg);
        if (PF_H == 2) { touch1(sws, 36864u + lane * 64u, hg); touch1(sws, 2u * 36864u + lane * 64u, hg); }
        if (PF_H == 3) {
#pragma unroll
          for (u32 k = 0; k < 4; ++k) touch1(sws, 36864u + (k >> 1) * 36864u + (k & 1u) * 2048u + lane * 32u, hg);
        }
      }
    }
    asm volatile("s_branch 2f\n\t.p2align 12\n2:" ::: "memory");
    const u32 T = __builtin_amdgcn_readfirstlane(*(volatile AS3 u32*)(L + LDS_FLAGS + F_T));
    const u32 nib = T & 15u;
    u32x2 bb = *(const u32x2*)(b2 + g * 64 + s * 16 + q * 4);
    asm volatile("" : "+v"(bb));
    auto lsum = [&](u32 mt, u32 t) {
      const lptr r = L + (mt < 3u ? (u32)w2area(2) + mt * 16384u : (mt - 3u) * 16384u) + t * 1024 + lane * 16;
      const f32x4 a0 = *(AS3 f32x4*)r, a1 = *(AS3 f32x4*)(r + 4096), a2 = *(AS3 f32x4*)(r + 8192), a3 = *(AS3 f32x4*)(r + 12288);
      return (a0 + a1) + (a2 + a3);
    };
    if (u != s) {
      const u32 pout = WS_P + (((mb * 9 + g) * 4 + u) * 4 + s) * 5120u;
#pragma unroll 1
      for (u32 mt = 0; mt < nm; ++mt) {
        wait1(L + LDS_FLAGS + F_PART + mt * 4, 0x01010101u);
        const u32x4 w = __builtin_bit_cast(u32x4, lsum(mt, u));
        __builtin_amdgcn_raw_buffer_store_b128((w & ~15u) | nib, rw, lane * 16, pout + mt * 1024, 16);
      }
    }
    const u32 pin = WS_P + ((mb * 9 + g) * 4 + s) * 4 * 5120u;
    u32 so[3];
#pragma unroll
    for (u32 k = 0; k < 3; ++k) so[k] = pin + ((s + 1u + k) & 3u) * 5120u;
    const __amdgpu_buffer_rsrc_t ro = __builtin_amdgcn_make_buffer_rsrc(out, 0, 589824u, 0x00020000);
    const u32 oo = (mt0 * 16u + (lane & 15u)) * 1152u + (g * 64u + s * 16u + q * 4u) * 2u;
    u32 mt = (u - s) & 3u;
    if (mt == 0u || mt + 1u < nm) {
#pragma unroll 1
      for (;;) {
        wait1(L + LDS_FLAGS + F_PART + mt * 4, 0x01010101u);
        const u32 vi = lane * 16 + mt * 1024;
        u32x4 p[3], r[3];
        auto ld = [&](u32x4* d) {
#pragma unroll
          for (int k = 0; k < 3; ++k) d[k] = __builtin_amdgcn_raw_buffer_load_b128(rw, vi, so[k], 16);
          asm volatile("" ::: "memory");
        };
        auto ok = [&](const u32x4* d) {
          u32 bad = 0;
#pragma unroll
          for (int k = 0; k < 3; ++k) bad |= (d[k].x ^ nib) | (d[k].y ^ nib) | (d[k].z ^ nib) | (d[k].w ^ nib);
          return !any_lane((bad & 15u) != 0u);
        };
        ld(p);
        f32x4 v = lsum(mt, s);
        asm volatile("" : "+v"(v));
        __builtin_amdgcn_s_sleep(POLL_GAP);
#pragma unroll 1
        for (int it = 0; it < SPIN_MAX; ++it) {
          ld(r);
          if (ok(p)) break;
          ld(p);
          if (ok(r)) { p[0] = r[0]; p[1] = r[1]; p[2] = r[2]; break; }
        }
#pragma unroll
        for (int k = 0; k < 3; ++k) v += __builtin_bit_cast(f32x4, p[k] & ~15u);
        const u32 p0 = pk(v.x, v.y), p1 = pk(v.z, v.w);
        u32x2 o;
        o.x = pk(lo(p0) + lo(bb.x), hi(p0) + hi(bb.x));
        o.y = pk(lo(p1) + lo(bb.y), hi(p1) + hi(bb.y));
        __builtin_amdgcn_raw_buffer_store_b64(o, ro, oo, mt * (16 * 1152), OUT_AUX);
        if (mt != 0u) break;
        mt = nm - 1u;
      }
      if (u == 0u && s == 0u && g == 0u && lane == 0u) __builtin_amdgcn_raw_buffer_store_b32(T, rw, 0, mb * 256, 16);
    }
    return;
  }

  const u32 w = (u32)wave - 4u;
  if (CPRIO) __builtin_amdgcn_s_setprio(CPRIO);
  const u32 a = (lane >> 2) & 3, p = lane & 3, m = lane & 15;
  const u32x2 bA = *(const u32x2*)(b1 + (s * 9 + g) * 128 + w * 32 + q * 4);
  const u32x2 bB = *(const u32x2*)(b1 + (s * 9 + g) * 128 + w * 32 + 16 + q * 4);
  const u32 Ev = __builtin_amdgcn_raw_buffer_load_b32(rw, 0, mb * 256, 16);
  f32x4 acc[2][5];
#pragma unroll
  for (int t = 0; t < 2; ++t)
#pragma unroll
    for (int mt = 0; mt < 5; ++mt) acc[t][mt] = (f32x4){0.f, 0.f, 0.f, 0.f};
  {
    const u32 xa0 = m * 128u + (q ^ (m >> 1)) * 16u;
    const u32 wa0 = 10240u + (8 * q + a) * 256u + ((2 * w) ^ (a | ((q & 1) << 2))) * 32u + p * 8u;
    bf16x8 A[2][2], B[2][5];
    auto rd = [&](int ks, u32 slot) {
#pragma unroll
      for (int t = 0; t < 2; ++t) {
        const lptr pw = L + (slot + (wa0 ^ (t * 32u))) + ks * 8192;
        A[ks][t] = tr2(pw, pw + 1024);
      }
#pragma unroll
      for (int mt = 0; mt < 5; ++mt) B[ks][mt] = *(AS3 bf16x8*)(L + (slot + (xa0 ^ (ks * 64u))) + mt * 2048);
    };
    auto mm = [&](int ks) {
#pragma unroll
      for (int mt = 0; mt < 5; ++mt)
#pragma unroll
        for (int t = 0; t < 2; ++t) acc[t][mt] = __builtin_amdgcn_mfma_f32_16x16x32_bf16(A[ks][t], B[ks][mt], acc[t][mt], 0, 0, 0);
    };
    auto mix = [&]() {
#pragma unroll
      for (int k = 0; k < 2; ++k) { __builtin_amdgcn_sched_group_barrier(0x008, 1, 0); __builtin_amdgcn_sched_group_barrier(0x100, 2, 0); }
#pragma unroll
      for (int k = 0; k < 5; ++k) { __builtin_amdgcn_sched_group_barrier(0x008, 1, 0); __builtin_amdgcn_sched_group_barrier(0x100, 1, 0); }
      __builtin_amdgcn_sched_group_barrier(0x008, 3, 0);
    };
    u32 slot = 0;
    BAR();
    rd(0, 0u);
#pragma unroll 1
    for (u32 j = 0; j < 9; ++j) {
      __builtin_amdgcn_sched_barrier(0);
      rd(1, slot); mm(0); mix();
      __builtin_amdgcn_sched_barrier(0);
      asm volatile("s_cmp_eq_u32 %0, 8\n\ts_cbranch_scc1 1f\n\ts_barrier\n\t1:" :: "s"(j) : "memory", "scc");
      slot = slot == 5u * SLOT ? 0u : slot + SLOT;
      __builtin_amdgcn_sched_barrier(0);
      rd(0, slot); mm(1); mix();
      __builtin_amdgcn_sched_barrier(0);
    }
    __builtin_amdgcn_sched_barrier(0);
  }
  asm volatile("s_branch 2f\n\t" PAGE(".Lpg0") "2:\n\ts_waitcnt lgkmcnt(0)" ::: "memory");
  LDSW(F_P1 + w * 4, 1u);
  const u32 T = ~__builtin_amdgcn_readfirstlane(Ev);
  const i32x4 sws = make_srd(ws, WS_END);
  const f32x2 bv[4] = {{lo(bA.x), hi(bA.x)}, {lo(bA.y), hi(bA.y)}, {lo(bB.x), hi(bB.x)}, {lo(bB.y), hi(bB.y)}};
  u32 vo = lane * 16u;
  const u32 S = ((T >> 15) & 1u) * 0x80008000u;
  const u32 hb = WS_H + (mb * 4 + s) * (5u * 36864u) + w * 1024u;
  u32 so[9];
#pragma unroll
  for (u32 i = 0; i < 9; ++i) so[i] = hb + (g + i >= 9 ? g + i - 9 : g + i) * 4096u;
  bf16x8 W[9][4];
  u32x4 H[9];
  u32 have = 0;
  const u32 wb = (32 * w + 4 * q + a) * 128u + ((2 * q + (a >> 1)) & 3) * 32u + p * 8u;
  const u32 lw = w * 4096u + lane * 16u;
  auto rdw = [&](int i) {
    wait2(L + LDS_FLAGS + F_W2, (u32)i + 1u, have);
    const int base = i < 4 ? w2area(0) : i == 4 ? w2area(4) : 0;
#pragma unroll
    for (int t = 0; t < 4; ++t) {
      u32 pa = (wb ^ (t * 32u)) + base;
      asm("" : "+v"(pa));
      const lptr pw = L + pa + (w2area(i) - base);
      W[i][t] = tr2(pw, pw + 2048);
    }
  };
  u32x4 hp;
#pragma unroll
  for (int i = 0; i < 5; ++i) {
    u32x4 h;
    h.x = act2(acc[0][i].x, acc[0][i].y, bv[0]); h.y = act2(acc[0][i].z, acc[0][i].w, bv[1]);
    h.z = act2(acc[1][i].x, acc[1][i].y, bv[2]); h.w = act2(acc[1][i].z, acc[1][i].w, bv[3]);
    asm volatile("buffer_store_dwordx4 %0, %1, %2, %3 offen sc1\n\ts_nop 1" :: "v"(h | S), "v"((i < 4 || nm == 5u) ? vo + i * 36864u : 0x80000000u), "s"(sws), "s"(so[0]) : "memory");
    if (i == 0) { hp = h; for (int k = 0; k < WQ0; ++k) rdw(k); }
    if (i == 1) { for (int k = WQ0; k < WQ1; ++k) rdw(k); *(AS3 u32x4*)(L + w2area(0) + lw) = hp; }
    if (i == 3) { for (int k = WQ1; k < WQ3; ++k) rdw(k); }
    if (i >= 1) *(AS3 u32x4*)(L + (i < 4 ? w2area(0) + i * 1024 : w2area(1)) + lw) = h;
    __builtin_amdgcn_sched_barrier(0);
  }
  HLOAD(1, 224, 227); HLOAD(2, 228, 231); HLOAD(3, 232, 235); HLOAD(4, 236, 239);
  HLOAD(5, 240, 243); HLOAD(6, 244, 247); HLOAD(7, 248, 251); HLOAD(8, 252, 255);
  if (PF_H) { WAITV(10); LDSW(F_ACK + w * 4, 1u); }
#pragma unroll
  for (int k = WQ3; k < 9; ++k) rdw(k);
  u32 cnt = SPIN_MAX, t, first = 1, mt = 0;
  u32 ra = (u32)w2area(2) + lw, oh = (u32)w2area(0) + lw, fa = LDS_FLAGS + F_PART + w;
  u32 vn = vo + 36864u;
#define GC(a, b, c, d) "v_bitop3_b32 %[t1], v" #a ", v" #b ", %[S] bitop3:0x7e\n\tv_bitop3_b32 %[t2], v" #c ", v" #d ", %[S] bitop3:0x7e\n\tv_or3_b32 %[t], %[t], %[t1], %[t2]\n\t"
#define GL(a, b, k) "buffer_load_dwordx4 v[" #a ":" #b "], %[vr], %[rw], %[s" #k "] offen sc1\n\t"
#pragma unroll 1
  for (;;) {
    u32 t1, t2;
    asm volatile("s_cmp_eq_u32 %[first], 0\n\ts_cbranch_scc1 3f\n\t"
                 "1:\n\ts_waitcnt vmcnt(0)\n\tv_mov_b32 %[t], 0\n\t"
                 GC(224, 225, 226, 227) GC(228, 229, 230, 231) GC(232, 233, 234, 235) GC(236, 237, 238, 239)
                 GC(240, 241, 242, 243) GC(244, 245, 246, 247) GC(248, 249, 250, 251) GC(252, 253, 254, 255)
                 "v_and_b32 %[t], %[M], %[t]\n\tv_cmp_ne_u32 vcc, 0, %[t]\n\ts_cbranch_vccz 2f\n\t"
                 "s_cmp_eq_u32 %[cnt], 0\n\ts_cbranch_scc1 2f\n\t3:\n\ts_sub_u32 %[cnt], %[cnt], 1\n\t"
                 GL(224, 227, 1) GL(228, 231, 2) GL(232, 235, 3) GL(236, 239, 4) GL(240, 243, 5) GL(244, 247, 6) GL(248, 251, 7) GL(252, 255, 8)
                 "s_branch 1b\n\t" PAGE(".Lpg1") "2:\n\t" X2(224, 225) X2(226, 227) "s_nop 1"
                 : [t] "=&v"(t), [t1] "=&v"(t1), [t2] "=&v"(t2), [cnt] "+s"(cnt), "+{v[224:227]}"(H[1]), "+{v[228:231]}"(H[2]),
                   "+{v[232:235]}"(H[3]), "+{v[236:239]}"(H[4]), "+{v[240:243]}"(H[5]), "+{v[244:247]}"(H[6]), "+{v[248:251]}"(H[7]), "+{v[252:255]}"(H[8])
                 : [S] "s"(S), [M] "s"(0x80008000u), [vr] "v"(vo), [rw] "s"(sws), [s1] "s"(so[1]), [s2] "s"(so[2]), [s3] "s"(so[3]), [s4] "s"(so[4]),
                   [s5] "s"(so[5]), [s6] "s"(so[6]), [s7] "s"(so[7]), [s8] "s"(so[8]), [first] "s"(first)
                 : "memory", "vcc", "scc");
    if (PF_H) LDSW(F_ACK + w * 4, 2u);
    first = 0;
    f32x4 o[4];
    STEP_X(1, 224, 227, 228, 229, 230, 231, "0", "0", "0", "0", "=&v");
    bool done = false;
#pragma unroll 1
    for (;;) {
      asm volatile("ds_read_b128 %0, %1" : "=v"(H[0]) : "v"(oh) : "memory");
      STEP_X(2, 228, 231, 232, 233, 234, 235, ACC);
      STEP_X(3, 232, 235, 236, 237, 238, 239, ACC);
      STEP_X(4, 236, 239, 240, 241, 242, 243, ACC);
      STEP_X(5, 240, 243, 244, 245, 246, 247, ACC);
      STEP_X(6, 244, 247, 248, 249, 250, 251, ACC);
      STEP_X(7, 248, 251, 252, 253, 254, 255, ACC);
      u64 bad;
      asm volatile(MF("%[o0]", "%[w0]", "v[252:255]", "%[o0]") "v_and_b32 %[t], %[M], %[t]\n\ts_waitcnt lgkmcnt(0)\n\t"
                   MF("%[o0]", "%[u0]", "%[h]", "%[o0]") "v_cmp_ne_u32 %[bad], 0, %[t]\n\t" WV6
                   MF("%[o1]", "%[w1]", "v[252:255]", "%[o1]") X2(224, 225)
                   MF("%[o1]", "%[u1]", "%[h]", "%[o1]") X2(226, 227)
                   MF("%[o2]", "%[w2]", "v[252:255]", "%[o2]") DW("%[o0]", 0)
                   MF("%[o2]", "%[u2]", "%[h]", "%[o2]") "v_or_b32 %[t], v224, v225\n\tv_or3_b32 %[t], %[t], v226, v227\n\t"
                   MF("%[o3]", "%[w3]", "v[252:255]", "%[o3]") RF(252, 255) DW("%[o1]", 1024)
                   MF("%[o3]", "%[u3]", "%[h]", "%[o3]")
                   : [o0] "+v"(o[0]), [o1] "+v"(o[1]), [o2] "+v"(o[2]), [o3] "+v"(o[3]), [t] "+v"(t), [bad] "=&s"(bad),
                     "+{v[252:255]}"(H[8]), "+{v[224:227]}"(H[1])
                   : WOPS(8), [u0] "v"(W[0][0]), [u1] "v"(W[0][1]), [u2] "v"(W[0][2]), [u3] "v"(W[0][3]), [h] "v"(H[0]),
                     [S] "s"(S), [M] "s"(0x80008000u), [vn] "v"(vn), [rw] "s"(sws), [sc] "s"(so[8]), [ra] "v"(ra)
                   : "memory");
      if (__builtin_expect(bad != 0ull && cnt != 0u, 0)) break;
      ++mt;
      vo = vn;
      vn = mt + 1u < nm ? vn + 36864u : 0x80000000u;
      asm volatile(MF("%[o0]", "%[w0]", "v[224:227]", "0") DW("%[o2]", 2048) WV6 X2(228, 229)
                   MF("%[o1]", "%[w1]", "v[224:227]", "0") X2(230, 231)
                   MF("%[o2]", "%[w2]", "v[224:227]", "0") DW("%[o3]", 3072) "ds_write_b8 %[fa], %[one]\n\t" O2(228, 229, 230, 231)
                   MF("%[o3]", "%[w3]", "v[224:227]", "0") RF(224, 227)
                   : [o0] "+v"(o[0]), [o1] "+v"(o[1]), [o2] "+v"(o[2]), [o3] "+v"(o[3]), [t] "+v"(t),
                     "+{v[224:227]}"(H[1]), "+{v[228:231]}"(H[2])
                   : WOPS(1), [S] "s"(S), [vn] "v"(vn), [rw] "s"(sws), [sc] "s"(so[1]), [ra] "v"(ra), [fa] "v"(fa), [one] "v"(1u)
                   : "memory");
      if (mt == nm) { done = true; break; }
      ra += mt == 3u ? 0u - (u32)w2area(4) : 16384u;
      oh += mt == 4u ? 16384u - 3072u : 1024u;
      fa += 4u;
    }
    if (done) break;
  }
  asm volatile(".Lffn_tail:");
}

extern "C" void kernel_launch(void* const* d_in, const int* in_sizes, int n_in, void* d_out, int out_size, void* d_ws,
                              size_t ws_size, hipStream_t stream) {
  (void)in_sizes; (void)n_in; (void)out_size; (void)ws_size;
  (void)hipFuncSetAttribute((const void*)ffn_kernel, hipFuncAttributeMaxDynamicSharedMemorySize, LDS_BYTES);
  ffn_kernel<<<256, 512, LDS_BYTES, stream>>>((const u16*)d_in[0], (const u16*)d_in[1], (const u16*)d_in[2],
                                              (const u16*)d_in[3], (const u16*)d_in[4], (u16*)d_out, (char*)d_ws);
}
```
